```python
import torch

def _softmax(x, axis=-1):
    return torch.softmax(x, dim=axis)
def _gelu(x):
    return torch.nn.functional.gelu(x, approximate="tanh")
def _silu(x):
    return torch.nn.functional.silu(x)
def _sigmoid(x):
    return torch.sigmoid(x)
def _layernorm(x, axis=-1, eps=1e-5):
    m = x.mean(dim=axis, keepdim=True)
    v = x.var(dim=axis, keepdim=True, unbiased=False)
    return (x - m) / torch.sqrt(v + eps)
def _scatter_add(tgt, idx, src, axis=0):
    return tgt.clone().index_add_(axis, idx, src)
def _segment_sum(src, seg, n):
    out = torch.zeros((n,) + src.shape[1:], dtype=src.dtype)
    return out.index_add_(0, seg, src)
def _pad(x, pw):
    flat = tuple(v for pair in reversed(pw) for v in pair)
    return torch.nn.functional.pad(x, flat)

kernel_name = 'gated_mix__softmax_full_mha__dense_activation_mish__prompt'

def setup_inputs():

    _w = (lambda a: (lambda t: t.to(torch.bfloat16) if t.is_floating_point() else t)(torch.as_tensor(a)))
    def _A_setup():
        B, S, D = 1, 2048, 512
        return {
            "x": _w((lambda *s: torch.randn(*s, dtype=torch.bfloat16) * 0.05)(B, S, D)),
            "wqkv": _w((lambda *s: torch.randn(*s, dtype=torch.bfloat16) * 0.8)(D, 3 * D)),
            "wo": _w((lambda *s: torch.randn(*s, dtype=torch.bfloat16) * 0.05)(D, D)),
        }
    _A_in = _A_setup()

    def _B_setup():
        B, S, D, F = 1, 2048, 512, 1376
        return {
            "x": _w((lambda *s: torch.randn(*s, dtype=torch.bfloat16) * 0.05)(B, S, D)),
            "w1": _w((lambda *s: torch.randn(*s, dtype=torch.bfloat16) * 12.8)(D, F) * (D ** -0.5)),
            "b1": _w((lambda *s: torch.randn(*s, dtype=torch.bfloat16) * 0.2)(F)),
            "w2": _w((lambda *s: torch.randn(*s, dtype=torch.bfloat16) * 0.1)(F, D) * (F ** -0.5)),
        }
    _B_in = _B_setup()

    return {
        "A_x": _A_in["x"],
        "A_wqkv": _A_in["wqkv"],
        "A_wo": _A_in["wo"],
        "B_x": _B_in["x"],
        "B_w1": _B_in["w1"],
        "B_b1": _B_in["b1"],
        "B_w2": _B_in["w2"],
        "gate_w": _w((lambda *s: torch.randn(*s, dtype=torch.bfloat16) * 0.05)(512) * 16.0),
    }

def reference(A_x, A_wqkv, A_wo, B_x, B_w1, B_b1, B_w2, gate_w):
    def _A_ref(x, wqkv, wo):
        B, S, D = x.shape
        H = 32; Dh = D // H
        qkv = (lambda x, *d: x.permute(*d))((x @ wqkv).reshape(B, S, 3, H, Dh), 2, 0, 3, 1, 4)
        q, k, v = qkv[0], qkv[1], qkv[2]
        scale = Dh ** -0.5
        scores = torch.einsum("bhqd,bhkd->bhqk", q, k) * scale
        p = _softmax(scores, axis=-1)
        o = torch.einsum("bhqk,bhkd->bhqd", p, v)
        return (lambda x, *d: x.permute(*d))(o, 0, 2, 1, 3).reshape(B, S, D) @ wo

    def _B_ref(x, w1, b1, w2):
        h = torch.einsum("bsd,df->bsf", x, w1) + b1
        sp = torch.log1p(torch.exp(-torch.abs(h))) + torch.where(h > 0, h, torch.zeros_like(h))
        act = h * torch.tanh(sp)
        return torch.einsum("bsf,fd->bsd", act, w2)

    x = A_x
    g = _sigmoid((x * gate_w).sum(-1, keepdims=True))
    a = _A_ref(x, A_wqkv, A_wo)
    b = _B_ref(x, B_w1, B_b1, B_w2)
    return g * a + (1 - g) * b
```

```cpp
#include <hip/hip_runtime.h>
#include <hip/hip_bf16.h>
#include <stdint.h>

using bf16x8 = __attribute__((ext_vector_type(8))) short;
using s16x4  = __attribute__((ext_vector_type(4))) short;
using f32x16 = __attribute__((ext_vector_type(16))) float;
using f32x4  = __attribute__((ext_vector_type(4))) float;
using u32x4  = __attribute__((ext_vector_type(4))) unsigned;
using u32x2  = __attribute__((ext_vector_type(2))) unsigned;
typedef short v4i16_t __attribute__((ext_vector_type(4)));
typedef __attribute__((address_space(3))) const char* lds_cptr;
typedef unsigned short u16;

#define DI __device__ __forceinline__
#define MFMA(a, b, c) __builtin_amdgcn_mfma_f32_32x32x16_bf16((a), (b), (c), 0, 0, 0)

constexpr int SEQ = 2048, DM = 512, NH = 32, DH = 16, FF = 1376, NQKV = 1536;
constexpr int NTT = SEQ / 32;
constexpr int FKS = FF / 16;
constexpr float QSCALE = 0.25f * 1.4426950408889634f;

constexpr size_t WS_Q = 0;
constexpr size_t WS_K = WS_Q + (size_t)SEQ * DM * 2;
constexpr size_t WS_V = WS_K + (size_t)SEQ * DM * 2;
constexpr size_t WS_O = WS_V + (size_t)SEQ * DM * 2;
constexpr size_t WS_ACT = WS_O + (size_t)SEQ * DM * 2;
constexpr size_t WS_KST = WS_ACT + (size_t)SEQ * FF * 2;
constexpr size_t WS_G = WS_KST + (size_t)NH * NTT * 4;

DI unsigned cvtpk(float lo, float hi) { unsigned r; asm("v_cvt_pk_bf16_f32 %0, %1, %2" : "=v"(r) : "v"(lo), "v"(hi)); return r; }
DI float bf2f(u16 v) { return __uint_as_float((unsigned)v << 16); }
DI float bflo(unsigned v) { return __uint_as_float(v << 16); }
DI float bfhi(unsigned v) { return __uint_as_float(v & 0xffff0000u); }
DI void glds16(const void* g, unsigned lds_base) {
    unsigned sv;
    asm volatile("s_mov_b32 %0, m0\n\ts_mov_b32 m0, %2\n\ts_nop 0\n\tglobal_load_lds_dwordx4 %1, off\n\ts_mov_b32 m0, %0"
                 : "=&s"(sv) : "v"(g), "s"(lds_base) : "memory");
}
DI s16x4 trread(lds_cptr p) { return __builtin_bit_cast(s16x4, __builtin_amdgcn_ds_read_tr16_b64_v4i16((__attribute__((address_space(3))) v4i16_t*)p)); }
DI bf16x8 ldsfrag(lds_cptr p) { return *(const __attribute__((address_space(3))) bf16x8*)p; }
DI bf16x8 cat8(s16x4 a, s16x4 b) { return __builtin_shufflevector(a, b, 0, 1, 2, 3, 4, 5, 6, 7); }
#define WAITV(n) asm volatile("s_waitcnt vmcnt(" #n ")" ::: "memory")
template <int N> DI void wait_vm_bar() { asm volatile("s_waitcnt vmcnt(%0) lgkmcnt(0)\n\ts_barrier" :: "i"(N) : "memory"); }
DI float xor32f(float v) {
    auto r = __builtin_amdgcn_permlane32_swap(__float_as_uint(v), __float_as_uint(v), false, false);
    return (threadIdx.x & 32) ? __uint_as_float(r[0]) : __uint_as_float(r[1]);
}
DI float wave_max(float v) {
#pragma unroll
    for (int o = 32; o >= 1; o >>= 1) v = fmaxf(v, __shfl_xor(v, o));
    return v;
}
DI float wave_sum(float v) {
#pragma unroll
    for (int o = 32; o >= 1; o >>= 1) v += __shfl_xor(v, o);
    return v;
}
DI float mish_f(float h) {
    float t = __builtin_amdgcn_exp2f(-fabsf(h) * 1.4426950408889634f);
    float t2 = t * t;
    float num = h > 0.f ? 1.f + 2.f * t : t2 + 2.f * t;
    float den = h > 0.f ? num + 2.f * t2 : num + 2.f;
    return h * num * __builtin_amdgcn_rcpf(den);
}

constexpr int P_BM = 128, P_BN = 96, P_BK = 64, P_NST = 2;
constexpr int P_ASZ = P_BM * P_BK * 2, P_WSZ = P_BK * P_BN * 2, P_STG = P_ASZ + P_WSZ;
constexpr int P_NK = DM / P_BK;
constexpr int P_NT_QKV = NQKV / P_BN;
constexpr int P_NT_FF = (FF + P_BN - 1) / P_BN;

template <int VMASK>
DI void proj_mainloop(f32x16 (&acc)[3], const char* lds, unsigned lds0, const u16* xsrc, const u16* const (&wsrc)[3], int ldw,
                      int wid, int lane) {
    const int r = lane & 31, hi = lane >> 5;
    const int arow = wid * 32 + r;
    const int asw = (arow >> 1) & 7;
    const int g = lane >> 4, i16 = lane & 15;
    const int wofs = ((g >> 1) * 8 + (i16 >> 2)) * (P_BN * 2) + ((g & 1) * 16 + (i16 & 3) * 4) * 2;
    auto issue = [&](int ks, int buf) {
        const unsigned base = lds0 + buf * P_STG;
#pragma unroll
        for (int j = 0; j < 4; ++j) glds16(xsrc + (size_t)j * 32 * DM + ks * P_BK, base + (j * 4 + wid) * 1024);
#pragma unroll
        for (int j = 0; j < 3; ++j) glds16(wsrc[j] + (size_t)ks * P_BK * ldw, base + P_ASZ + (j * 4 + wid) * 1024);
    };
    issue(0, 0);
    issue(1, 1);
#pragma unroll 1
    for (int ks = 0; ks < P_NK; ++ks) {
        const int buf = ks & 1;
        if (ks + 1 < P_NK) wait_vm_bar<7>(); else wait_vm_bar<0>();
        lds_cptr A = (lds_cptr)lds + buf * P_STG + arow * 128;
        lds_cptr W = (lds_cptr)lds + buf * P_STG + P_ASZ + wofs;
#pragma unroll
        for (int kk = 0; kk < 4; ++kk) {
            bf16x8 xf = ldsfrag(A + (((2 * kk + hi) ^ asw) << 4));
#pragma unroll
            for (int n = 0; n < 3; ++n) {
                bf16x8 wf = cat8(trread(W + kk * 16 * (P_BN * 2) + n * 64), trread(W + (kk * 16 + 4) * (P_BN * 2) + n * 64));
                if ((VMASK >> n) & 1) acc[n] = MFMA(xf, wf, acc[n]); else acc[n] = MFMA(wf, xf, acc[n]);
            }
        }
        if (ks + 2 < P_NK) {
            asm volatile("s_waitcnt lgkmcnt(0)\n\ts_barrier" ::: "memory");
            issue(ks + 2, buf);
        }
    }
}

__global__ __launch_bounds__(256, 2) void proj_kernel(const u16* __restrict__ x, const u16* __restrict__ wqkv, const u16* __restrict__ w1,
                                                      const u16* __restrict__ b1, const u16* __restrict__ gate_w, char* __restrict__ ws) {
    extern __shared__ __attribute__((aligned(16))) char lds[];
    const int tid = threadIdx.x, lane = tid & 63, wid = __builtin_amdgcn_readfirstlane(tid >> 6);
    const int r = lane & 31, hi = lane >> 5;
    const int mt = blockIdx.x & 15, nt = blockIdx.x >> 4;
    const bool isff = nt >= P_NT_QKV;
    const int n0 = (isff ? nt - P_NT_QKV : nt) * P_BN;
    const int ldw = isff ? FF : NQKV;
    const u16* W = isff ? w1 : wqkv;
    const int m0 = mt * P_BM;
    const unsigned lds0 = (unsigned)(uintptr_t)lds;

    u32x4 gx0, gx1, gw;
    if (!isff) {
        const int t0 = m0 + nt * 8 + wid * 2;
        gx0 = *(const u32x4*)(x + (size_t)t0 * DM + lane * 8);
        gx1 = *(const u32x4*)(x + (size_t)(t0 + 1) * DM + lane * 8);
        gw = *(const u32x4*)(gate_w + lane * 8);
    }
    const int prow = wid * 8 + (lane >> 3);
    const int pch = (lane & 7) ^ ((prow >> 1) & 7);
    const u16* xsrc = x + (size_t)(m0 + prow) * DM + pch * 8;
    const u16* wsrc[3];
#pragma unroll
    for (int j = 0; j < 3; ++j) {
        const int o = (j * 4 + wid) * 1024 + lane * 16;
        const int k = o / (P_BN * 2), c = (o % (P_BN * 2)) >> 4;
        int n = n0 + c * 8;
        n = n > ldw - 8 ? ldw - 8 : n;
        wsrc[j] = W + (size_t)k * ldw + n;
    }
    f32x16 acc[3];
#pragma unroll
    for (int n = 0; n < 3; ++n) acc[n] = f32x16{};
    const int vmask = isff ? 0 : (nt > 10 ? 7 : (nt == 10 ? 4 : 0));
    if (vmask == 0) proj_mainloop<0>(acc, lds, lds0, xsrc, wsrc, ldw, wid, lane);
    else if (vmask == 7) proj_mainloop<7>(acc, lds, lds0, xsrc, wsrc, ldw, wid, lane);
    else proj_mainloop<4>(acc, lds, lds0, xsrc, wsrc, ldw, wid, lane);

    const int tt = mt * 4 + wid;
    if (!isff) {
#pragma unroll
        for (int n = 0; n < 3; ++n) {
            const int sj = nt * 3 + n;
            const int sec = sj >> 4, h0 = (sj & 15) * 2;
            if (sec == 0) {
#pragma unroll
                for (int e = 0; e < 2; ++e) {
                    u32x4 p;
#pragma unroll
                    for (int q = 0; q < 4; ++q) p[q] = cvtpk(acc[n][8 * e + 2 * q] * QSCALE, acc[n][8 * e + 2 * q + 1] * QSCALE);
                    *(u32x4*)(ws + WS_Q + ((size_t)((h0 + e) * NTT + tt) * 64 + lane) * 16) = p;
                }
            } else if (sec == 1) {
#pragma unroll
                for (int e = 0; e < 2; ++e) {
                    u32x4 p;
                    float ss = 0.f;
#pragma unroll
                    for (int q = 0; q < 4; ++q) {
                        p[q] = cvtpk(acc[n][8 * e + 2 * q], acc[n][8 * e + 2 * q + 1]);
                        const float a = bflo(p[q]), b = bfhi(p[q]);
                        ss = fmaf(a, a, ss); ss = fmaf(b, b, ss);
                    }
                    *(u32x4*)(ws + WS_K + ((size_t)((h0 + e) * NTT + tt) * 64 + lane) * 16) = p;
                    ss += xor32f(ss);
                    float bad = (ss == ss) ? 0.f : 1.f;
                    ss = wave_max(ss); bad = wave_max(bad);
                    if (lane == 0) ((float*)(ws + WS_KST))[(h0 + e) * NTT + tt] = bad > 0.f ? __builtin_inff() : ss;
                }
            } else {
                const int hh = h0 + (r >> 4), d = r & 15;
#pragma unroll
                for (int s = 0; s < 2; ++s) {
                    u32x4 p;
#pragma unroll
                    for (int q = 0; q < 4; ++q) p[q] = cvtpk(acc[n][8 * s + 2 * q], acc[n][8 * s + 2 * q + 1]);
                    *(u32x4*)(ws + WS_V + (size_t)(hh * NTT + tt) * 1024 + s * 512 + hi * 256 + d * 16) = p;
                }
            }
        }
        float s0 = 0.f, s1 = 0.f;
#pragma unroll
        for (int q = 0; q < 4; ++q) {
            s0 = fmaf(bflo(gx0[q]), bflo(gw[q]), s0); s0 = fmaf(bfhi(gx0[q]), bfhi(gw[q]), s0);
            s1 = fmaf(bflo(gx1[q]), bflo(gw[q]), s1); s1 = fmaf(bfhi(gx1[q]), bfhi(gw[q]), s1);
        }
        s0 = wave_sum(s0); s1 = wave_sum(s1);
        if (lane < 2) {
            const float s = lane ? s1 : s0;
            ((float*)(ws + WS_G))[m0 + nt * 8 + wid * 2 + lane] = 1.f / (1.f + __expf(-s));
        }
    } else {
#pragma unroll
        for (int n = 0; n < 3; ++n) {
            const int f0 = n0 + n * 32;
            if (f0 < FF) {
#pragma unroll
                for (int e = 0; e < 2; ++e) {
                    const u32x2 ba = *(const u32x2*)(b1 + f0 + 16 * e + 4 * hi);
                    const u32x2 bb = *(const u32x2*)(b1 + f0 + 16 * e + 4 * hi + 8);
                    float v[8];
                    v[0] = mish_f(acc[n][8 * e + 0] + bflo(ba[0])); v[1] = mish_f(acc[n][8 * e + 1] + bfhi(ba[0]));
                    v[2] = mish_f(acc[n][8 * e + 2] + bflo(ba[1])); v[3] = mish_f(acc[n][8 * e + 3] + bfhi(ba[1]));
                    v[4] = mish_f(acc[n][8 * e + 4] + bflo(bb[0])); v[5] = mish_f(acc[n][8 * e + 5] + bfhi(bb[0]));
                    v[6] = mish_f(acc[n][8 * e + 6] + bflo(bb[1])); v[7] = mish_f(acc[n][8 * e + 7] + bfhi(bb[1]));
                    u32x4 p;
#pragma unroll
                    for (int q = 0; q < 4; ++q) p[q] = cvtpk(v[2 * q], v[2 * q + 1]);
                    *(u32x4*)(ws + WS_ACT + ((size_t)(tt * FKS + (f0 >> 4) + e) * 64 + lane) * 16) = p;
                }
            }
        }
    }
}

constexpr int A_LK = 0, A_LV = 65536, A_LC = 131072, A_CSZ = 8192, A_LDS = A_LC + A_CSZ;
constexpr float A_THR = 40.f;

DI u32x4 pack_trunc(const f32x16& p, int s) {
    u32x4 r;
#pragma unroll
    for (int q = 0; q < 4; ++q) r[q] = __builtin_amdgcn_perm(__float_as_uint(p[8 * s + 2 * q + 1]), __float_as_uint(p[8 * s + 2 * q]), 0x07060302u);
    return r;
}

__global__ __launch_bounds__(512, 2) void attn_kernel(char* __restrict__ ws) {
    extern __shared__ __attribute__((aligned(16))) char lds[];
    const int tid = threadIdx.x, lane = tid & 63, wid = __builtin_amdgcn_readfirstlane(tid >> 6);
    const int r = lane & 31, hi = lane >> 5;
    const int b = blockIdx.x;
    const int head = (b & 7) * 4 + ((b >> 3) & 3), qg = b >> 5;
    const int qt = qg * 8 + wid;
    const unsigned lds0 = (unsigned)(uintptr_t)lds;

    const bf16x8 qf = *(const bf16x8*)(ws + WS_Q + ((size_t)(head * NTT + qt) * 64 + lane) * 16);
    const float kst = ((const float*)(ws + WS_KST))[head * NTT + lane];
    {
        const char* ksrc = ws + WS_K + ((size_t)(head * NTT + wid) * 64 + lane) * 16;
        const char* vsrc = ws + WS_V + ((size_t)(head * NTT + wid) * 64 + lane) * 16;
#pragma unroll
        for (int c = 0; c < 8; ++c) {
            glds16(ksrc + c * 8192, lds0 + A_LK + (c * 8 + wid) * 1024);
            glds16(vsrc + c * 8192, lds0 + A_LV + (c * 8 + wid) * 1024);
        }
    }
    {
        const unsigned one2 = ((tid & 15) == 0 || (tid & 15) == 4) ? 0x3f803f80u : 0u;
        *(__attribute__((address_space(3))) u32x4*)((__attribute__((address_space(3))) char*)lds + A_LC + tid * 16) = u32x4{one2, one2, one2, one2};
    }
    float qs = 0.f;
#pragma unroll
    for (int j = 0; j < 8; ++j) { const float a = bf2f((u16)qf[j]); qs = fmaf(a, a, qs); }
    qs += xor32f(qs);
    const float kmax = wave_max(kst);
    const float mref = sqrtf(qs) * sqrtf(kmax) * 1.001f;
    const bool fast = __all(mref <= A_THR);

    f32x16 o = f32x16{};
    lds_cptr kp = (lds_cptr)lds + A_LK + lane * 16;
    lds_cptr vp = (lds_cptr)lds + (r < 16 ? A_LV + hi * 256 + r * 16 : A_LC + hi * 256 + (r - 16) * 16);
    const int vinc = r < 16 ? 8192 : 0;

    if (fast) {
        f32x16 negm;
#pragma unroll
        for (int i = 0; i < 16; ++i) negm[i] = -mref;
#define CHUNK(c)                                                                        \
        {                                                                               \
            wait_vm_bar<2 * (7 - (c))>();                                               \
            _Pragma("unroll")                                                           \
            for (int t = 0; t < 8; ++t) {                                               \
                const bf16x8 kf = ldsfrag(kp + (c) * 8192 + t * 1024);                  \
                const bf16x8 v0 = ldsfrag(vp + t * 1024), v1 = ldsfrag(vp + t * 1024 + 512); \
                f32x16 s = MFMA(kf, qf, negm);                                          \
                _Pragma("unroll")                                                       \
                for (int i = 0; i < 16; ++i) s[i] = __builtin_amdgcn_exp2f(s[i]);       \
                o = MFMA(v0, __builtin_bit_cast(bf16x8, pack_trunc(s, 0)), o);          \
                o = MFMA(v1, __builtin_bit_cast(bf16x8, pack_trunc(s, 1)), o);          \
            }                                                                           \
            vp += vinc;                                                                 \
        }
        CHUNK(0) CHUNK(1) CHUNK(2) CHUNK(3) CHUNK(4) CHUNK(5) CHUNK(6) CHUNK(7)
#undef CHUNK
    } else {
        float m = -__builtin_inff();
        f32x16 zero = f32x16{};
#define CHUNK(c)                                                                        \
        {                                                                               \
            wait_vm_bar<2 * (7 - (c))>();                                               \
            _Pragma("unroll 1")                                                         \
            for (int t = 0; t < 8; ++t) {                                               \
                const bf16x8 kf = ldsfrag(kp + (c) * 8192 + t * 1024);                  \
                const bf16x8 v0 = ldsfrag(vp + t * 1024), v1 = ldsfrag(vp + t * 1024 + 512); \
                f32x16 s = MFMA(kf, qf, zero);                                          \
                float mx = s[0];                                                        \
                _Pragma("unroll")                                                       \
                for (int i = 1; i < 16; ++i) mx = fmaxf(mx, s[i]);                      \
                mx = fmaxf(mx, xor32f(mx));                                             \
                const float mn = fmaxf(m, mx);                                          \
                const float al = __builtin_amdgcn_exp2f(m - mn);                        \
                m = mn;                                                                 \
                _Pragma("unroll")                                                       \
                for (int i = 0; i < 16; ++i) { s[i] = __builtin_amdgcn_exp2f(s[i] - mn); o[i] *= al; } \
                o = MFMA(v0, __builtin_bit_cast(bf16x8, pack_trunc(s, 0)), o);          \
                o = MFMA(v1, __builtin_bit_cast(bf16x8, pack_trunc(s, 1)), o);          \
            }                                                                           \
            vp += vinc;                                                                 \
        }
        CHUNK(0) CHUNK(1) CHUNK(2) CHUNK(3) CHUNK(4) CHUNK(5) CHUNK(6) CHUNK(7)
#undef CHUNK
    }
    const float rl = 1.f / o[8];
    u32x4 p;
#pragma unroll
    for (int q = 0; q < 4; ++q) p[q] = cvtpk(o[2 * q] * rl, o[2 * q + 1] * rl);
    *(u32x4*)(ws + WS_O + ((size_t)(qt * NH + head) * 64 + lane) * 16) = p;
}

constexpr int O_NST = 6, O_STG = 16384, O_NS1 = DM / 64, O_NS2 = (FF + 63) / 64, O_NS = O_NS1 + O_NS2;

__global__ __launch_bounds__(256, 1) void out_kernel(const u16* __restrict__ wo, const u16* __restrict__ w2, const char* __restrict__ ws, u16* __restrict__ out) {
    extern __shared__ __attribute__((aligned(16))) char lds[];
    const int tid = threadIdx.x, lane = tid & 63, wid = __builtin_amdgcn_readfirstlane(tid >> 6);
    const int r = lane & 31, hi = lane >> 5;
    const int mt = blockIdx.x >> 3, nt = blockIdx.x & 7;
    const int wm = wid >> 1, wn = wid & 1;
    const unsigned lds0 = (unsigned)(uintptr_t)lds;
    const float g = ((const float*)(ws + WS_G))[mt * 64 + wm * 32 + r];

    const char* aO = ws + WS_O + ((size_t)((mt * 2 + (wid >> 1)) * NH + (wid & 1) * 2) * 64 + lane) * 16;
    const char* aF = ws + WS_ACT + ((size_t)((mt * 2 + (wid >> 1)) * FKS + (wid & 1) * 2) * 64 + lane) * 16;
    const int wk = (wid * 2) * 8 + (lane >> 5) * 4 + ((lane >> 2) & 3);
    const int wc = nt * 64 + ((lane >> 4) & 1) * 32 + (lane & 3) * 8;
    auto issue = [&](int s, int buf) {
        const unsigned base = lds0 + buf * O_STG;
        if (s < O_NS1) {
            glds16(aO + (size_t)s * 4096, base + (wid * 2) * 1024);
            glds16(aO + (size_t)s * 4096 + 1024, base + (wid * 2 + 1) * 1024);
            glds16(wo + (size_t)(s * 64 + wk) * DM + wc, base + 8192 + (wid * 2) * 1024);
            glds16(wo + (size_t)(s * 64 + wk + 8) * DM + wc, base + 8192 + (wid * 2 + 1) * 1024);
        } else {
            const int s2 = s - O_NS1;
            const int ks = s2 * 4 + (wid & 1) * 2;
            const int adj = ks + 1 < FKS ? 0 : -2;
            glds16(aF + (size_t)(s2 * 4 + adj) * 1024, base + (wid * 2) * 1024);
            glds16(aF + (size_t)(s2 * 4 + adj + 1) * 1024, base + (wid * 2 + 1) * 1024);
            int k0 = s2 * 64 + wk, k1 = k0 + 8;
            k0 = k0 < FF ? k0 : FF - 1; k1 = k1 < FF ? k1 : FF - 1;
            glds16(w2 + (size_t)k0 * DM + wc, base + 8192 + (wid * 2) * 1024);
            glds16(w2 + (size_t)k1 * DM + wc, base + 8192 + (wid * 2 + 1) * 1024);
        }
    };
#pragma unroll
    for (int s = 0; s < O_NST - 1; ++s) issue(s, s);
    f32x16 acca = f32x16{}, accb = f32x16{};
    const int i16 = lane & 15;
    const int wofs = 8192 + (hi * 2 + wn) * 256 + (i16 >> 2) * 64 + ((lane >> 4) & 1) * 32 + (i16 & 3) * 8;
    const int aofs = wm * 4096 + lane * 16;
    int buf = 0, ibuf = O_NST - 1;
#pragma unroll 1
    for (int s = 0; s < O_NS; ++s) {
        if (s + O_NST - 2 < O_NS) wait_vm_bar<4 * (O_NST - 2)>(); else wait_vm_bar<0>();
        if (s + O_NST - 1 < O_NS) issue(s + O_NST - 1, ibuf);
        lds_cptr A = (lds_cptr)lds + buf * O_STG + aofs;
        lds_cptr W = (lds_cptr)lds + buf * O_STG + wofs;
        if (s < O_NS1) {
#pragma unroll
            for (int kk = 0; kk < 4; ++kk)
                acca = MFMA(cat8(trread(W + kk * 2048), trread(W + kk * 2048 + 1024)), ldsfrag(A + kk * 1024), acca);
        } else {
#pragma unroll
            for (int kk = 0; kk < 2; ++kk)
                accb = MFMA(cat8(trread(W + kk * 2048), trread(W + kk * 2048 + 1024)), ldsfrag(A + kk * 1024), accb);
            if (s + 1 < O_NS) {
#pragma unroll
                for (int kk = 2; kk < 4; ++kk)
                    accb = MFMA(cat8(trread(W + kk * 2048), trread(W + kk * 2048 + 1024)), ldsfrag(A + kk * 1024), accb);
            }
        }
        buf = buf + 1 == O_NST ? 0 : buf + 1;
        ibuf = ibuf + 1 == O_NST ? 0 : ibuf + 1;
    }
    const float g1 = 1.f - g;
    u16* orow = out + (size_t)(mt * 64 + wm * 32 + r) * DM + nt * 64 + wn * 32 + hi * 4;
#pragma unroll
    for (int q = 0; q < 4; ++q) {
        u32x2 p;
        p[0] = cvtpk(g * acca[4 * q] + g1 * accb[4 * q], g * acca[4 * q + 1] + g1 * accb[4 * q + 1]);
        p[1] = cvtpk(g * acca[4 * q + 2] + g1 * accb[4 * q + 2], g * acca[4 * q + 3] + g1 * accb[4 * q + 3]);
        *(u32x2*)(orow + q * 8) = p;
    }
}

extern "C" void kernel_launch(void* const* d_in, const int* in_sizes, int n_in, void* d_out, int out_size, void* d_ws, size_t ws_size,
                              hipStream_t stream) {
    const u16* x = (const u16*)d_in[0];
    const u16* wqkv = (const u16*)d_in[1];
    const u16* wo = (const u16*)d_in[2];
    const u16* w1 = (const u16*)d_in[4];
    const u16* b1 = (const u16*)d_in[5];
    const u16* w2 = (const u16*)d_in[6];
    const u16* gw = (const u16*)d_in[7];
    char* ws = (char*)d_ws;
    (void)hipFuncSetAttribute((const void*)attn_kernel, hipFuncAttributeMaxDynamicSharedMemorySize, A_LDS);
    (void)hipFuncSetAttribute((const void*)out_kernel, hipFuncAttributeMaxDynamicSharedMemorySize, O_NST * O_STG);
    proj_kernel<<<16 * (P_NT_QKV + P_NT_FF), 256, P_NST * P_STG, stream>>>(x, wqkv, w1, b1, gw, ws);
    attn_kernel<<<256, 512, A_LDS, stream>>>(ws);
    out_kernel<<<256, 256, O_NST * O_STG, stream>>>(wo, w2, ws, (u16*)d_out);
}
```

```cpp
#include <hip/hip_runtime.h>
#include <hip/hip_bf16.h>
#include <stdint.h>

using bf16x8 = __attribute__((ext_vector_type(8))) short;
using s16x4  = __attribute__((ext_vector_type(4))) short;
using f32x16 = __attribute__((ext_vector_type(16))) float;
using f32x4  = __attribute__((ext_vector_type(4))) float;
using f32x2  = __attribute__((ext_vector_type(2))) float;
using u32x4  = __attribute__((ext_vector_type(4))) unsigned;
using u32x2  = __attribute__((ext_vector_type(2))) unsigned;
typedef __bf16 bf16x2_t __attribute__((ext_vector_type(2)));
typedef short v4i16_t __attribute__((ext_vector_type(4)));
typedef __attribute__((address_space(3))) const char* lds_cptr;
typedef __attribute__((address_space(3))) char* lds_ptr;
typedef unsigned short u16;

#define DI __device__ __forceinline__
#define TS(i)
#define TSEND(k)
#define MFMA(a, b, c) __builtin_amdgcn_mfma_f32_32x32x16_bf16((a), (b), (c), 0, 0, 0)
#define SBAR() __builtin_amdgcn_sched_barrier(0)
#define PIN(x) asm volatile("" : "+v"(x))

constexpr int SEQ = 2048, DM = 512, NH = 32, DH = 16, FF = 1376, NQKV = 1536;
constexpr int NTT = SEQ / 32;
constexpr int FKS = FF / 16;
constexpr float QSCALE = 0.25f * 1.4426950408889634f;

constexpr size_t WS_Q = 0;
constexpr size_t WS_K = WS_Q + (size_t)SEQ * DM * 2;
constexpr size_t WS_V = WS_K + (size_t)SEQ * DM * 2;
constexpr size_t WS_O = WS_V + (size_t)SEQ * DM * 2;
constexpr size_t WS_ACT = WS_O + (size_t)SEQ * DM * 2;
constexpr size_t WS_KST = WS_ACT + (size_t)SEQ * FF * 2;
constexpr size_t WS_VST = WS_KST + (size_t)NH * NTT * 4;
constexpr size_t WS_G = WS_VST + (size_t)16 * NTT * 4;
constexpr size_t WS_FQ = WS_G + (size_t)SEQ * 4;
constexpr size_t WS_FA = WS_FQ + 16 * 256;
constexpr size_t WS_FO = WS_FA + 16 * 256;
constexpr size_t WS_EP = WS_FO + 8 * 256;
constexpr size_t WS_SCR = WS_EP + 256;

DI unsigned cvtpk(float lo, float hi) { f32x2 v = {lo, hi}; return __builtin_bit_cast(unsigned, __builtin_convertvector(v, bf16x2_t)); }
DI float bf2f(u16 v) { return __uint_as_float((unsigned)v << 16); }
DI float bflo(unsigned v) { return __uint_as_float(v << 16); }
DI float bfhi(unsigned v) { return __uint_as_float(v & 0xffff0000u); }
DI void glds16(const void* sbase, unsigned voff, unsigned lds_base) {
    unsigned sv;
    asm volatile("s_mov_b32 %0, m0\n\ts_mov_b32 m0, %3\n\ts_nop 0\n\tglobal_load_lds_dwordx4 %1, %2\n\ts_mov_b32 m0, %0"
                 : "=&s"(sv) : "v"(voff), "s"(sbase), "s"(lds_base) : "memory");
}
template <int OFF> DI void gload16(bf16x8& dst, const void* sbase, unsigned voff) {
    asm volatile("global_load_dwordx4 %0, %1, %2 offset:%3" : "=v"(dst) : "v"(voff), "s"(sbase), "i"(OFF) : "memory");
}
DI s16x4 trread(lds_cptr p) { return __builtin_bit_cast(s16x4, __builtin_amdgcn_ds_read_tr16_b64_v4i16((__attribute__((address_space(3))) v4i16_t*)p)); }
DI bf16x8 ldsfrag(lds_cptr p) { return *(const __attribute__((address_space(3))) bf16x8*)p; }
DI bf16x8 cat8(s16x4 a, s16x4 b) { return __builtin_shufflevector(a, b, 0, 1, 2, 3, 4, 5, 6, 7); }
template <int N> DI void wait_vm_bar() { asm volatile("s_waitcnt vmcnt(%0)\n\ts_barrier" :: "i"(N) : "memory"); }
template <int N> DI void wait_vm_lgkm_bar() { asm volatile("s_waitcnt vmcnt(%0) lgkmcnt(0)\n\ts_barrier" :: "i"(N) : "memory"); }
DI float xor32f(float v) {
    auto r = __builtin_amdgcn_permlane32_swap(__float_as_uint(v), __float_as_uint(v), false, false);
    return (threadIdx.x & 32) ? __uint_as_float(r[0]) : __uint_as_float(r[1]);
}
DI float dpp_max63(float v) {
#define DSTEP(ctrl, rmask) v = fmaxf(v, __int_as_float(__builtin_amdgcn_update_dpp(__float_as_int(v), __float_as_int(v), ctrl, rmask, 0xf, false)))
    DSTEP(0x111, 0xf); DSTEP(0x112, 0xf); DSTEP(0x114, 0xf); DSTEP(0x118, 0xf); DSTEP(0x142, 0xa); DSTEP(0x143, 0xc);
#undef DSTEP
    return v;
}
DI float dpp_sum63(float v) {
#define DSTEP(ctrl, rmask) v += __int_as_float(__builtin_amdgcn_update_dpp(0, __float_as_int(v), ctrl, rmask, 0xf, false))
    DSTEP(0x111, 0xf); DSTEP(0x112, 0xf); DSTEP(0x114, 0xf); DSTEP(0x118, 0xf); DSTEP(0x142, 0xa); DSTEP(0x143, 0xc);
#undef DSTEP
    return v;
}
DI float bcast63(float v) { return __int_as_float(__builtin_amdgcn_readlane(__float_as_int(v), 63)); }
DI float mish_f(float h) {
    float t = __builtin_amdgcn_exp2f(-fabsf(h) * 1.4426950408889634f);
    float t2 = t * t;
    float num = h > 0.f ? 1.f + 2.f * t : t2 + 2.f * t;
    float den = h > 0.f ? num + 2.f * t2 : num + 2.f;
    return h * num * __builtin_amdgcn_rcpf(den);
}

typedef __attribute__((address_space(1))) unsigned gu32;
#define RLX_AGENT __ATOMIC_RELAXED, __HIP_MEMORY_SCOPE_AGENT
DI void st16_sc1(void* sbase, unsigned voff, u32x4 v) {
    asm volatile("global_store_dwordx4 %0, %1, %2 sc1\n\ts_nop 1" :: "v"(voff), "v"(v), "s"(sbase) : "memory");
}
DI void st4_sc1(void* p, float v) { __hip_atomic_store((gu32*)p, __float_as_uint(v), RLX_AGENT); }
DI void publish(void* flag, unsigned tag) {
    asm volatile("s_waitcnt vmcnt(0) lgkmcnt(0)\n\ts_barrier" ::: "memory");
    if (threadIdx.x == 0) __hip_atomic_store((gu32*)flag, tag, RLX_AGENT);
}
DI void wait_flags(const void* flags, int n, unsigned tag, int lane) {
    for (unsigned spins = 0; spins < (1u << 18); ++spins) {
        unsigned v = tag;
        if (lane < n) v = __hip_atomic_load((const gu32*)flags + lane, RLX_AGENT);
        if (__all(v == tag)) break;
        __builtin_amdgcn_s_sleep(1);
    }
    __builtin_amdgcn_fence(__ATOMIC_ACQUIRE, "agent");
    asm volatile("s_waitcnt vmcnt(0)" ::: "memory");
}

#define ROT (((idx >> 2) + 2 * (idx & 3)) & 7)
constexpr int P_BN = 96, P_ROW = P_BN * 2;
constexpr int G_XS = 16384, G_STG = G_XS + 64 * P_ROW, G_NSL = 4, G_EX = G_NSL * G_STG, LDS_BYTES = G_EX + 49152;
constexpr int A_SLOT = 32768, A_LC = 2 * A_SLOT, A_CSZ = 8192, A_LQ = A_LC + A_CSZ, A_LDS = A_LQ + 9216;
constexpr int M_STG = 16384, M_NSL = 10, M_NS1 = (FF + 63) / 64, M_NS = M_NS1 + DM / 64;
constexpr int M_PRE = 4;
constexpr float A_THR = 40.f;
static_assert(A_LDS <= 6 * M_STG && G_EX >= A_LDS, "lds map");

DI bf16x8 pack8(const f32x16& p, int s) {
    u32x4 r;
#pragma unroll
    for (int q = 0; q < 4; ++q) r[q] = cvtpk(p[8 * s + 2 * q], p[8 * s + 2 * q + 1]);
    return __builtin_bit_cast(bf16x8, r);
}
#define LGKM_BAR() asm volatile("s_waitcnt lgkmcnt(0)\n\ts_barrier" ::: "memory")
DI void raise_flag(void* flag, unsigned tag) { if (threadIdx.x == 0) __hip_atomic_store((gu32*)flag, tag, RLX_AGENT); }

struct Frag { bf16x8 x[2]; bf16x8 w[2][3]; };
struct GCtx {
    const u16 *x, *wqkv, *w1; int rot; unsigned xoff, woq[2], wof[2], ldsx, ldsw0, ldsw1; lds_cptr lds; int xa0, xa1, wa0;
};
template <int S> DI void g_issue(const GCtx& c) {
    constexpr unsigned base = (S & 3) * G_STG;
    const int kb = (S + c.rot) & 7;
    const u16* xs = c.x + kb * 64;
    glds16(xs, c.xoff, c.ldsx + base);
    glds16(xs + 8 * DM, c.xoff ^ 64u, c.ldsx + base + 1024);
    if constexpr (S < 8) {
        const u16* wsrc = c.wqkv + (size_t)kb * 64 * NQKV;
        glds16(wsrc, c.woq[0], c.ldsw0 + base);
        glds16(wsrc, c.woq[1], c.ldsw1 + base);
    } else {
        const u16* wsrc = c.w1 + (size_t)kb * 64 * FF;
        glds16(wsrc, c.wof[0], c.ldsw0 + base);
        glds16(wsrc, c.wof[1], c.ldsw1 + base);
    }
}
template <int S> DI void g_rdfrag(const GCtx& c, Frag& f) {
    lds_cptr b = c.lds + (S & 3) * G_STG;
    f.x[0] = ldsfrag(b + c.xa0); f.x[1] = ldsfrag(b + c.xa1);
#pragma unroll
    for (int j = 0; j < 2; ++j)
#pragma unroll
        for (int n = 0; n < 3; ++n)
            f.w[j][n] = cat8(trread(b + c.wa0 + j * 16 * P_ROW + n * 64), trread(b + c.wa0 + (j * 16 + 4) * P_ROW + n * 64));
}
template <int S> DI void g_stage(const GCtx& c, f32x16 (&acc)[3], Frag& fc, Frag& fn) {
    if constexpr (S + 1 < 16) {
        constexpr int last = S + 3 < 16 ? S + 3 : 15;
        wait_vm_lgkm_bar<4 * (last - S - 1) + ((S >= 8 && S <= 10) ? 6 : 0)>();
        if constexpr (S + 4 < 16) g_issue<S + 4>(c);
        g_rdfrag<S + 1>(c, fn);
    }
    SBAR();
#pragma unroll
    for (int j = 0; j < 2; ++j) {
        acc[0] = MFMA(fc.w[j][0], fc.x[j], acc[0]);
        acc[1] = MFMA(fc.w[j][1], fc.x[j], acc[1]);
        if constexpr (S < 8) acc[2] = MFMA(fc.x[j], fc.w[j][2], acc[2]); else acc[2] = MFMA(fc.w[j][2], fc.x[j], acc[2]);
    }
    PIN(acc[0]); PIN(acc[1]); PIN(acc[2]);
    SBAR();
}

struct MCtx {
    const char *aF, *aO; const u16 *w2, *wo; unsigned l16, wo_l, ldsb; lds_cptr A, W; int lasta, lastw;
};
template <int S> DI void m_issue(const MCtx& c) {
    constexpr unsigned base = ((S + 6) % M_NSL) * M_STG;
    if constexpr (S < M_NS1 - 1) {
        glds16(c.aF + (size_t)S * 4096, c.l16, c.ldsb + base);
        glds16(c.w2 + (size_t)S * 64 * DM, c.wo_l, c.ldsb + base + 8192);
    } else if constexpr (S == M_NS1 - 1) {
        glds16(c.aF + (size_t)S * 4096 - c.lasta, c.l16, c.ldsb + base);
        glds16(c.w2 + (size_t)S * 64 * DM - c.lastw, c.wo_l, c.ldsb + base + 8192);
    } else {
        glds16(c.aO + (size_t)(S - M_NS1) * 4096, c.l16, c.ldsb + base);
        glds16(c.wo + (size_t)(S - M_NS1) * 64 * DM, c.wo_l, c.ldsb + base + 8192);
    }
}
struct MFrag { bf16x8 a0, a1, w0, w1; };
template <int S> DI void m_rdfrag(const MCtx& c, MFrag& f) {
    constexpr int base = ((S + 6) % M_NSL) * M_STG;
    f.w0 = cat8(trread(c.W + base), trread(c.W + base + 1024)); f.a0 = ldsfrag(c.A + base);
    f.w1 = cat8(trread(c.W + base + 2048), trread(c.W + base + 3072)); f.a1 = ldsfrag(c.A + base + 1024);
}
template <int S0, int S1> struct MIssueRange { static DI void run(const MCtx& c) { if constexpr (S0 < S1) { m_issue<S0>(c); MIssueRange<S0 + 1, S1>::run(c); } } };
constexpr int M_OS = 9;
constexpr int M_FS = 3;
struct MSync { char* ws; void* myflag; const void* oflags; unsigned tag; int lane, wid, kh; const float* gp; };
template <int S> struct MStage {
    static DI void run(const MCtx& c, const MSync& y, f32x16& acca, f32x16& accb, MFrag& fc, MFrag& fn, float& g) {
        if constexpr (S + 1 < M_NS) {
            if constexpr (S == M_OS) { if (y.wid == 0) wait_flags(y.oflags, 32, y.tag, y.lane); }
            constexpr int last = S + M_NSL - 1 < M_NS ? S + M_NSL - 1 : M_NS - 1;
            if constexpr (S == M_FS) wait_vm_lgkm_bar<4 + 2 * M_FS>(); else wait_vm_lgkm_bar<2 * (last - S - 1)>();
            if constexpr (S == M_FS) raise_flag(y.myflag, y.tag);
            if constexpr (S + M_NSL < M_NS) m_issue<S + M_NSL>(c);
            if constexpr (S == M_OS + 1) g = *y.gp;
            m_rdfrag<S + 1>(c, fn);
        }
        SBAR();
        if constexpr (S == M_NS1 - 1) { if (y.kh == 0) { accb = MFMA(fc.w0, fc.a0, accb); accb = MFMA(fc.w1, fc.a1, accb); } }
        else if constexpr (S < M_NS1) { accb = MFMA(fc.w0, fc.a0, accb); accb = MFMA(fc.w1, fc.a1, accb); PIN(accb); }
        else { acca = MFMA(fc.w0, fc.a0, acca); acca = MFMA(fc.w1, fc.a1, acca); PIN(acca); }
        SBAR();
        if constexpr (S + 1 < M_NS) MStage<S + 1>::run(c, y, acca, accb, fn, fc, g);
    }
};

__global__ __launch_bounds__(512, 2) void fused_kernel(const u16* __restrict__ x, const u16* __restrict__ wqkv, const u16* __restrict__ wo,
                                                       const u16* __restrict__ w1, const u16* __restrict__ b1, const u16* __restrict__ w2,
                                                       const u16* __restrict__ gate_w, char* __restrict__ ws, u16* __restrict__ out) {
    extern __shared__ __attribute__((aligned(16))) char lds[];
    const int tid = threadIdx.x, lane = tid & 63, wid = __builtin_amdgcn_readfirstlane(tid >> 6);
    const int r = lane & 31, hi = lane >> 5;
    const int xg = blockIdx.x & 7, idx = blockIdx.x >> 3;
    const unsigned lds0 = (unsigned)(uintptr_t)lds;
    const unsigned l16 = lane * 16;
    const int i16 = lane & 15, g4 = lane >> 4;
    TS(0);
    const int mt = (xg >> 1) * 4 + (idx & 3), nt = (xg & 1) * 8 + (idx >> 2);
    const int head = xg * 4 + (idx & 3), qg = idx >> 2;
    const int mt4 = xg * 4 + (idx & 3), nt4 = idx >> 2;
    unsigned tag;

    {
        const int tw = wid & 3, kh = wid >> 2;
        const int m0 = mt * 128;
        GCtx c;
        c.x = x; c.wqkv = wqkv; c.w1 = w1; c.lds = (lds_cptr)lds; c.rot = ROT;
#pragma unroll
        for (int j = 0; j < 2; ++j) {
            const int o = (j ? 8 + tw : wid) * 1024 + lane * 16;
            const int k = o / P_ROW, cc = (o % P_ROW) >> 4;
            c.woq[j] = (unsigned)(k * NQKV + (cc >> 2) * 512 + nt * 32 + (cc & 3) * 8) * 2u;
            int n = nt * P_BN + cc * 8;
            n = n > FF - 8 ? FF - 8 : n;
            c.wof[j] = (unsigned)(k * FF + n) * 2u;
        }
        c.xoff = (unsigned)((m0 + wid * 16 + (lane >> 3)) * DM) * 2u + (((lane & 7) ^ g4) << 4);
        c.ldsx = lds0 + wid * 2048; c.ldsw0 = lds0 + G_XS + wid * 1024; c.ldsw1 = lds0 + G_XS + 8192 + tw * 1024;
        const int gtok = m0 + nt * 8 + wid;
        unsigned epv; u32x4 gx, gw; u32x2 bia[3], bib[3];
        asm volatile("global_load_dword %0, %1, %2 sc1" : "=v"(epv) : "v"(0u), "s"(ws + WS_EP) : "memory");
        asm volatile("global_load_dwordx4 %0, %1, %2" : "=v"(gx) : "v"(l16), "s"(x + (size_t)gtok * DM) : "memory");
        asm volatile("global_load_dwordx4 %0, %1, %2" : "=v"(gw) : "v"(l16), "s"(gate_w) : "memory");
#pragma unroll
        for (int j = 0; j < 3; ++j) {
            int f = nt * P_BN + 16 * (3 * kh + j) + 4 * hi;
            f = f < FF ? f : 0;
            asm volatile("global_load_dwordx2 %0, %1, %2" : "=v"(bia[j]) : "v"((unsigned)f * 2u), "s"(b1) : "memory");
            asm volatile("global_load_dwordx2 %0, %1, %2 offset:16" : "=v"(bib[j]) : "v"((unsigned)f * 2u), "s"(b1) : "memory");
        }
        TS(13);
        g_issue<0>(c); g_issue<1>(c); g_issue<2>(c); g_issue<3>(c);
        TS(14);
        asm volatile("s_waitcnt vmcnt(12)\n\ts_barrier"
                     : "+v"(epv), "+v"(gx), "+v"(gw), "+v"(bia[0]), "+v"(bia[1]), "+v"(bia[2]), "+v"(bib[0]), "+v"(bib[1]), "+v"(bib[2]) :: "memory");
        const int xrow = tw * 32 + r, xf7 = (xrow >> 1) & 7;
        c.xa0 = xrow * 128 + (((4 * kh + hi) ^ xf7) << 4); c.xa1 = xrow * 128 + (((4 * kh + 2 + hi) ^ xf7) << 4);
        c.wa0 = G_XS + (kh * 32 + (g4 >> 1) * 8 + (i16 >> 2)) * P_ROW + ((g4 & 1) * 16 + (i16 & 3) * 4) * 2;
        f32x16 acc[3];
#pragma unroll
        for (int n = 0; n < 3; ++n) acc[n] = f32x16{};
        Frag fa, fb;
        g_rdfrag<0>(c, fa);
        tag = (unsigned)__builtin_amdgcn_readfirstlane(epv) * 0x9E3779B1u + 0x7F4A7C15u;
        float gs = 0.f;
#pragma unroll
        for (int q = 0; q < 4; ++q) { gs = fmaf(bflo(gx[q]), bflo(gw[q]), gs); gs = fmaf(bfhi(gx[q]), bfhi(gw[q]), gs); }
        TS(1);
        lds_ptr exw = (lds_ptr)lds + G_EX + wid * 6144 + lane * 16;
        lds_cptr exr = (lds_cptr)lds + G_EX + (wid ^ 4) * 6144 + lane * 16;
        float kp[24];
        auto ex_write = [&]() {
#pragma unroll
            for (int q = 0; q < 6; ++q) {
                f32x4 v;
#pragma unroll
                for (int e = 0; e < 4; ++e) { const int i = 4 * q + e; v[e] = kh ? acc[i >> 4][i & 15] : acc[(i + 24) >> 4][(i + 24) & 15]; kp[i] = kh ? acc[(i + 24) >> 4][(i + 24) & 15] : acc[i >> 4][i & 15]; }
                *(__attribute__((address_space(3))) f32x4*)(exw + q * 1024) = v;
            }
        };
        auto ex_read = [&]() {
#pragma unroll
            for (int q = 0; q < 6; ++q) {
                const f32x4 v = *(const __attribute__((address_space(3))) f32x4*)(exr + q * 1024);
#pragma unroll
                for (int e = 0; e < 4; ++e) kp[4 * q + e] += v[e];
            }
        };
        g_stage<0>(c, acc, fa, fb); g_stage<1>(c, acc, fb, fa); g_stage<2>(c, acc, fa, fb); g_stage<3>(c, acc, fb, fa);
        g_stage<4>(c, acc, fa, fb); g_stage<5>(c, acc, fb, fa); g_stage<6>(c, acc, fa, fb); g_stage<7>(c, acc, fb, fa);
        TS(2);
        const int tt = mt * 4 + tw;
        ex_write();
        LGKM_BAR();
        ex_read();
        {
            const int h0 = nt * 2;
            gs = dpp_sum63(gs);
            u32x4 p; float ss = 0.f;
#pragma unroll
            for (int q = 0; q < 4; ++q) {
                p[q] = kh ? cvtpk(kp[2 * q], kp[2 * q + 1]) : cvtpk(kp[16 + 2 * q], kp[16 + 2 * q + 1]);
                const float a = bflo(p[q]), b = bfhi(p[q]);
                ss = fmaf(a, a, ss); ss = fmaf(b, b, ss);
            }
            st16_sc1(ws + WS_K + (size_t)((h0 + kh) * NTT + tt) * 1024, l16, p);
            float vm = 0.f;
            if (kh == 0) {
#pragma unroll
                for (int e = 0; e < 2; ++e) {
                    u32x4 pq;
#pragma unroll
                    for (int q = 0; q < 4; ++q) pq[q] = cvtpk(kp[8 * e + 2 * q] * QSCALE, kp[8 * e + 2 * q + 1] * QSCALE);
                    st16_sc1(ws + WS_Q + (size_t)((h0 + e) * NTT + tt) * 1024, l16, pq);
                }
            } else {
                const unsigned vo = (unsigned)((r >> 4) * NTT * 1024 + hi * 256 + (r & 15) * 16);
#pragma unroll
                for (int s2 = 0; s2 < 2; ++s2) {
                    u32x4 pv;
#pragma unroll
                    for (int q = 0; q < 4; ++q) {
                        pv[q] = cvtpk(kp[8 + 8 * s2 + 2 * q], kp[8 + 8 * s2 + 2 * q + 1]);
                        const float a = kp[8 + 8 * s2 + 2 * q], b = kp[8 + 8 * s2 + 2 * q + 1];
                        vm = fmaxf(vm, fmaxf((a == a) ? fabsf(a) : __builtin_inff(), (b == b) ? fabsf(b) : __builtin_inff()));
                    }
                    st16_sc1(ws + WS_V + (size_t)(h0 * NTT + tt) * 1024 + s2 * 512, vo, pv);
                }
                vm = dpp_max63(vm);
            }
            ss += xor32f(ss);
            ss = (ss == ss) ? ss : __builtin_inff();
            ss = dpp_max63(ss);
            if (lane == 63) {
                st4_sc1(ws + WS_KST + (size_t)((h0 + kh) * NTT + tt) * 4, ss);
                st4_sc1(ws + (kh ? WS_VST + (size_t)(nt * NTT + tt) * 4 : WS_SCR + (size_t)(blockIdx.x * 8 + wid) * 1024), vm);
                st4_sc1(ws + WS_G + (size_t)gtok * 4, 1.f / (1.f + __expf(-gs)));
            }
        }
        TS(3);
        void* fq = ws + WS_FQ + nt * 256 + mt * 4;
        if (nt < 15) {
#pragma unroll
            for (int n = 0; n < 3; ++n) acc[n] = f32x16{};
            g_stage<8>(c, acc, fa, fb); g_stage<9>(c, acc, fb, fa);
            g_stage<10>(c, acc, fa, fb);
            g_stage<11>(c, acc, fb, fa);
            raise_flag(fq, tag);
            g_stage<12>(c, acc, fa, fb); g_stage<13>(c, acc, fb, fa);
            TS(11);
            if (wid == 0) wait_flags(ws + WS_FQ + (head >> 1) * 256, 16, tag, lane);
            TS(12);
            g_stage<14>(c, acc, fa, fb); g_stage<15>(c, acc, fb, fa);
            ex_write();
        } else {
            publish(fq, tag);
            if (wid == 0) wait_flags(ws + WS_FQ + (head >> 1) * 256, 16, tag, lane);
        }
        LGKM_BAR();
        TS(4);
        {
            const char* ksrc = ws + WS_K + (size_t)(head * NTT + wid) * 1024;
            const char* vsrc = ws + WS_V + (size_t)(head * NTT + wid) * 1024;
            glds16(ws + WS_Q + (size_t)(head * NTT + qg * 8 + wid) * 1024, l16, lds0 + A_LQ + wid * 1024);
            glds16(ws, (unsigned)(lane < 16 ? WS_KST + head * 256 + lane * 16 : WS_VST + (head >> 1) * 256 + (lane & 15) * 16), lds0 + A_LQ + 8192);
#pragma unroll
            for (int cc = 0; cc < 2; ++cc) {
                const unsigned dst = lds0 + cc * A_SLOT + wid * 1024;
                glds16(ksrc + cc * 8192, l16, dst);
                glds16(vsrc + cc * 8192, l16, dst + 8192);
                glds16(ksrc + cc * 8192 + 32768, l16, dst + 16384);
                glds16(vsrc + cc * 8192 + 32768, l16, dst + 24576);
            }
        }
        if (nt < 15) ex_read();
#pragma unroll
        for (int j = 0; j < 3; ++j) {
            const int f0 = nt * P_BN + 16 * (3 * kh + j);
            const u32x2 ba = bia[j], bb = bib[j];
            u32x4 p;
            p[0] = cvtpk(mish_f(kp[8 * j + 0] + bflo(ba[0])), mish_f(kp[8 * j + 1] + bfhi(ba[0])));
            p[1] = cvtpk(mish_f(kp[8 * j + 2] + bflo(ba[1])), mish_f(kp[8 * j + 3] + bfhi(ba[1])));
            p[2] = cvtpk(mish_f(kp[8 * j + 4] + bflo(bb[0])), mish_f(kp[8 * j + 5] + bfhi(bb[0])));
            p[3] = cvtpk(mish_f(kp[8 * j + 6] + bflo(bb[1])), mish_f(kp[8 * j + 7] + bfhi(bb[1])));
            st16_sc1(ws + (f0 < FF ? WS_ACT + (size_t)(tt * FKS + (f0 >> 4)) * 1024 : WS_SCR + (size_t)(blockIdx.x * 8 + wid) * 1024), l16, p);
        }
    }
    MCtx mc;
    mc.aF = ws + WS_ACT + (size_t)((mt4 * 2 + (wid >> 2)) * FKS + (wid & 3)) * 1024;
    mc.aO = ws + WS_O + (size_t)((mt4 * 2 + (wid >> 2)) * NH + (wid & 3)) * 1024;
    mc.w2 = w2; mc.wo = wo; mc.l16 = l16; mc.ldsb = lds0 + wid * 1024;
    mc.wo_l = (unsigned)((wid * 8 + hi * 4 + ((lane >> 2) & 3)) * DM + nt4 * 64 + (g4 & 1) * 32 + (lane & 3) * 8) * 2u;
    mc.lasta = (wid & 2) ? 2048 : 0; mc.lastw = wid >= 4 ? 32 * DM : 0;
    {
        const int qp = wid & 3, kh = wid >> 2;
        const int qt = qg * 8 + qp * 2;
        {
            const unsigned one2 = ((tid & 15) == 0 || (tid & 15) == 4) ? 0x3f803f80u : 0u;
            *(__attribute__((address_space(3))) u32x4*)((lds_ptr)lds + A_LC + tid * 16) = u32x4{one2, one2, one2, one2};
        }
        wait_vm_lgkm_bar<3 + 4>();
        TS(5);
        const bf16x8 qa = ldsfrag((lds_cptr)lds + A_LQ + qp * 2048 + lane * 16), qb = ldsfrag((lds_cptr)lds + A_LQ + qp * 2048 + 1024 + lane * 16);
        const float kst = *(const __attribute__((address_space(3))) float*)((lds_cptr)lds + A_LQ + 8192 + lane * 4);
        const float vst = *(const __attribute__((address_space(3))) float*)((lds_cptr)lds + A_LQ + 8192 + 256 + lane * 4);
        const char* ksrc = ws + WS_K + (size_t)(head * NTT + wid) * 1024;
        const char* vsrc = ws + WS_V + (size_t)(head * NTT + wid) * 1024;
        auto issue = [&](int cc) {
            const unsigned dst = lds0 + (cc & 1) * A_SLOT + wid * 1024;
            glds16(ksrc + cc * 8192, l16, dst);
            glds16(vsrc + cc * 8192, l16, dst + 8192);
            glds16(ksrc + cc * 8192 + 32768, l16, dst + 16384);
            glds16(vsrc + cc * 8192 + 32768, l16, dst + 24576);
        };
        float qs0 = 0.f, qs1 = 0.f;
#pragma unroll
        for (int j = 0; j < 8; ++j) { const float a = bf2f((u16)qa[j]), c2 = bf2f((u16)qb[j]); qs0 = fmaf(a, a, qs0); qs1 = fmaf(c2, c2, qs1); }
        qs0 += xor32f(qs0); qs1 += xor32f(qs1);
        const float kmax = bcast63(dpp_max63(kst)), vmax = bcast63(dpp_max63(vst));
        const float bound2 = fmaxf(qs0, qs1) * kmax * 1.01f;
        const bool fast = __all(bound2 <= A_THR * A_THR && qs0 == qs0 && qs1 == qs1 && vmax <= 1e18f);

        f32x16 oa = f32x16{}, ob = f32x16{};
        float ma = 0.f, mb = 0.f;
        const int kbase = kh * 16384 + lane * 16;
        const int vbase = r < 16 ? kh * 16384 + 8192 + hi * 256 + r * 16 : A_LC + hi * 256 + (r - 16) * 16;
        const int vmul = r < 16 ? A_SLOT : 0;
        const f32x16 zero = f32x16{};
        auto chunk_mid = [&](int cc) {
            if (cc < 3) wait_vm_bar<0>();
            if (cc == 0 && nt < 15) raise_flag(ws + WS_FA + mt * 256 + nt * 4, tag);
        };
        auto chunk_end = [&](int cc) {
            if (cc < 2) { wait_vm_lgkm_bar<0>(); issue(cc + 2); }
            if (cc == 1 && wid == 0) wait_flags(ws + WS_FA + (mt4 >> 1) * 256, 15, tag, lane);
            if (cc == 2) MIssueRange<0, M_PRE>::run(mc);
        };
        if (fast) {
            lds_cptr kc = (lds_cptr)lds + kbase, vc = (lds_cptr)lds + vbase;
            bf16x8 kf = ldsfrag(kc);
            bf16x8 va0 = ldsfrag(vc), va1 = ldsfrag(vc + 512), vb0, vb1;
            f32x16 sa = MFMA(kf, qa, zero), sb = MFMA(kf, qb, zero);
            kf = ldsfrag(kc + 1024);
#define HALF(S, O, V0, V1)                                                              \
                _Pragma("unroll")                                                       \
                for (int i = 0; i < 8; ++i) S[i] = __builtin_amdgcn_exp2f(S[i]);        \
                O = MFMA(V0, pack8(S, 0), O);                                           \
                PIN(O); SBAR();                                                         \
                _Pragma("unroll")                                                       \
                for (int i = 8; i < 16; ++i) S[i] = __builtin_amdgcn_exp2f(S[i]);       \
                O = MFMA(V1, pack8(S, 1), O);                                           \
                PIN(O); SBAR();
#define TILE(VC0, VC1, VN0, VN1, KNEXT, VNEXT)                                          \
            {                                                                           \
                VN0 = ldsfrag(VNEXT); VN1 = ldsfrag((VNEXT) + 512);                     \
                SBAR();                                                                 \
                HALF(sa, oa, VC0, VC1)                                                  \
                sa = MFMA(kf, qa, zero); PIN(sa); SBAR();                               \
                HALF(sb, ob, VC0, VC1)                                                  \
                sb = MFMA(kf, qb, zero); PIN(sb);                                       \
                kf = ldsfrag(KNEXT);                                                    \
                SBAR();                                                                 \
            }
#pragma unroll 1
            for (int cc = 0; cc < 4; ++cc) {
                const int ns = (cc + 1) & 1;
                lds_cptr kn = (lds_cptr)lds + kbase + ns * A_SLOT, vn = (lds_cptr)lds + vbase + ns * vmul;
                TILE(va0, va1, vb0, vb1, kc + 2048, vc + 1024)
                TILE(vb0, vb1, va0, va1, kc + 3072, vc + 2048)
                TILE(va0, va1, vb0, vb1, kc + 4096, vc + 3072)
                TILE(vb0, vb1, va0, va1, kc + 5120, vc + 4096)
                TILE(va0, va1, vb0, vb1, kc + 6144, vc + 5120)
                TILE(vb0, vb1, va0, va1, kc + 7168, vc + 6144)
                chunk_mid(cc);
                TILE(va0, va1, vb0, vb1, kn, vc + 7168)
                TILE(vb0, vb1, va0, va1, kn + 1024, vn)
                chunk_end(cc);
                kc = kn; vc = vn;
            }
#undef TILE
#undef HALF
        } else {
            ma = -__builtin_inff(); mb = ma;
#pragma unroll 1
            for (int cc = 0; cc < 4; ++cc) {
                lds_cptr kc = (lds_cptr)lds + kbase + (cc & 1) * A_SLOT, vc = (lds_cptr)lds + vbase + (cc & 1) * vmul;
#pragma unroll 1
                for (int t = 0; t < 8; ++t) {
                    if (t == 6) chunk_mid(cc);
                    const bf16x8 kf = ldsfrag(kc + t * 1024);
                    const bf16x8 v0 = ldsfrag(vc + t * 1024), v1 = ldsfrag(vc + t * 1024 + 512);
#define SLOW(Q, O, M)                                                                   \
                    {                                                                   \
                        f32x16 s = MFMA(kf, Q, zero);                                   \
                        float mx = s[0];                                                \
                        _Pragma("unroll")                                               \
                        for (int i = 1; i < 16; ++i) mx = fmaxf(mx, s[i]);              \
                        mx = fmaxf(mx, xor32f(mx));                                     \
                        const float mn = fmaxf(M, mx);                                  \
                        const float al = __builtin_amdgcn_exp2f(M - mn);                \
                        M = mn;                                                         \
                        _Pragma("unroll")                                               \
                        for (int i = 0; i < 16; ++i) { s[i] = __builtin_amdgcn_exp2f(s[i] - mn); O[i] *= al; } \
                        O = MFMA(v0, pack8(s, 0), O);                                   \
                        O = MFMA(v1, pack8(s, 1), O);                                   \
                    }
                    SLOW(qa, oa, ma)
                    SLOW(qb, ob, mb)
#undef SLOW
                }
                chunk_end(cc);
            }
        }
        TS(6);
        const f32x16& og = kh ? oa : ob;
        const float mg = kh ? ma : mb;
        lds_ptr red = (lds_ptr)lds + A_LC + wid * 3072 + lane * 16;
        LGKM_BAR();
        MIssueRange<M_PRE, M_NSL - 2>::run(mc);
        *(__attribute__((address_space(3))) f32x4*)(red) = f32x4{og[0], og[1], og[2], og[3]};
        *(__attribute__((address_space(3))) f32x4*)(red + 1024) = f32x4{og[4], og[5], og[6], og[7]};
        *(__attribute__((address_space(3))) f32x4*)(red + 2048) = f32x4{og[8], mg, 0.f, 0.f};
        LGKM_BAR();
        lds_cptr src = (lds_cptr)lds + A_LC + (wid ^ 4) * 3072 + lane * 16;
        const f32x4 x0 = *(const __attribute__((address_space(3))) f32x4*)(src);
        const f32x4 x1 = *(const __attribute__((address_space(3))) f32x4*)(src + 1024);
        const f32x4 x2 = *(const __attribute__((address_space(3))) f32x4*)(src + 2048);
        const f32x16& ok = kh ? ob : oa;
        const float mk = kh ? mb : ma;
        const float mm = fmaxf(mk, x2[1]);
        const float fk = __builtin_amdgcn_exp2f(mk - mm), fx = __builtin_amdgcn_exp2f(x2[1] - mm);
        const float rl = 1.f / (ok[8] * fk + x2[0] * fx);
        const float ck = fk * rl, cx = fx * rl;
        u32x4 p;
        p[0] = cvtpk(ok[0] * ck + x0[0] * cx, ok[1] * ck + x0[1] * cx);
        p[1] = cvtpk(ok[2] * ck + x0[2] * cx, ok[3] * ck + x0[3] * cx);
        p[2] = cvtpk(ok[4] * ck + x1[0] * cx, ok[5] * ck + x1[1] * cx);
        p[3] = cvtpk(ok[6] * ck + x1[2] * cx, ok[7] * ck + x1[3] * cx);
        st16_sc1(ws + WS_O + (size_t)((qt + kh) * NH + head) * 1024, l16, p);
        LGKM_BAR();
        MIssueRange<M_NSL - 2, M_NSL>::run(mc);
        TS(7);
    }
    {
        const int wm = wid & 1, wn = (wid >> 1) & 1, kh = wid >> 2;
        f32x16 acca = f32x16{}, accb = f32x16{};
        mc.W = (lds_cptr)lds + 8192 + kh * 4096 + (hi * 2 + wn) * 256 + (i16 >> 2) * 64 + (g4 & 1) * 32 + (i16 & 3) * 8;
        mc.A = (lds_cptr)lds + wm * 4096 + kh * 2048 + lane * 16;
        MSync y; y.ws = ws; y.myflag = ws + WS_FO + qg * 256 + head * 4; y.oflags = ws + WS_FO + (mt4 >> 2) * 256; y.tag = tag; y.lane = lane; y.wid = wid; y.kh = kh;
        y.gp = (const float*)(ws + WS_G) + mt4 * 64 + wm * 32 + r;
        float g = 0.f;
        MFrag fa, fb;
        wait_vm_bar<2 * (M_NSL - 1)>();
        m_rdfrag<0>(mc, fa);
        MStage<0>::run(mc, y, acca, accb, fa, fb, g);
        TS(9);
        const float g1 = 1.f - g;
        f32x16 v;
#pragma unroll
        for (int i = 0; i < 16; ++i) v[i] = g * acca[i] + g1 * accb[i];
        lds_ptr red = (lds_ptr)lds + wid * 2048 + lane * 16;
#pragma unroll
        for (int q = 0; q < 2; ++q) {
            f32x4 t;
#pragma unroll
            for (int e = 0; e < 4; ++e) t[e] = kh ? v[4 * q + e] : v[8 + 4 * q + e];
            *(__attribute__((address_space(3))) f32x4*)(red + q * 1024) = t;
        }
        LGKM_BAR();
        lds_cptr src = (lds_cptr)lds + (wid ^ 4) * 2048 + lane * 16;
        u16* orow = out + (size_t)(mt4 * 64 + wm * 32 + r) * DM + nt4 * 64 + wn * 32 + hi * 4 + kh * 16;
#pragma unroll
        for (int q = 0; q < 2; ++q) {
            const f32x4 t = *(const __attribute__((address_space(3))) f32x4*)(src + q * 1024);
            float o4[4];
#pragma unroll
            for (int e = 0; e < 4; ++e) o4[e] = (kh ? v[8 + 4 * q + e] : v[4 * q + e]) + t[e];
            u32x2 p; p[0] = cvtpk(o4[0], o4[1]); p[1] = cvtpk(o4[2], o4[3]);
            *(u32x2*)(orow + q * 8) = p;
        }
    }
    if (blockIdx.x == 0 && tid == 0) __hip_atomic_store((gu32*)(ws + WS_EP), tag, RLX_AGENT);
    TS(10);
}

extern "C" void kernel_launch(void* const* d_in, const int* in_sizes, int n_in, void* d_out, int out_size, void* d_ws, size_t ws_size,
                              hipStream_t stream) {
    (void)hipFuncSetAttribute((const void*)fused_kernel, hipFuncAttributeMaxDynamicSharedMemorySize, LDS_BYTES);
    fused_kernel<<<256, 512, LDS_BYTES, stream>>>((const u16*)d_in[0], (const u16*)d_in[1], (const u16*)d_in[2], (const u16*)d_in[4],
                                                  (const u16*)d_in[5], (const u16*)d_in[6], (const u16*)d_in[7], (char*)d_ws, (u16*)d_out);
}
```

```cpp
#include <hip/hip_runtime.h>
#include <hip/hip_bf16.h>
#include <stdint.h>

using bf16x8 = __attribute__((ext_vector_type(8))) short;
using s16x4  = __attribute__((ext_vector_type(4))) short;
using f32x16 = __attribute__((ext_vector_type(16))) float;
using f32x4  = __attribute__((ext_vector_type(4))) float;
using f32x2  = __attribute__((ext_vector_type(2))) float;
using u32x4  = __attribute__((ext_vector_type(4))) unsigned;
using u32x2  = __attribute__((ext_vector_type(2))) unsigned;
typedef __bf16 bf16x2_t __attribute__((ext_vector_type(2)));
typedef short v4i16_t __attribute__((ext_vector_type(4)));
typedef __attribute__((address_space(3))) const char* lds_cptr;
typedef __attribute__((address_space(3))) char* lds_ptr;
typedef unsigned short u16;

#define DI __device__ __forceinline__
#define TS(i)
#define TSEND(k)
#define MFMA(a, b, c) __builtin_amdgcn_mfma_f32_32x32x16_bf16((a), (b), (c), 0, 0, 0)
#define SBAR() __builtin_amdgcn_sched_barrier(0)
#define PIN(x) asm volatile("" : "+v"(x))

constexpr int SEQ = 2048, DM = 512, NH = 32, DH = 16, FF = 1376, NQKV = 1536;
constexpr int NTT = SEQ / 32;
constexpr int FKS = FF / 16;
constexpr float QSCALE = 0.25f * 1.4426950408889634f;

constexpr size_t WS_Q = 0;
constexpr size_t WS_K = WS_Q + (size_t)SEQ * DM * 2;
constexpr size_t WS_V = WS_K + (size_t)SEQ * DM * 2;
constexpr size_t WS_O = WS_V + (size_t)SEQ * DM * 2;
constexpr size_t WS_ACT = WS_O + (size_t)SEQ * DM * 2;
constexpr size_t WS_KST = WS_ACT + (size_t)SEQ * FF * 2;
constexpr size_t WS_VST = WS_KST + (size_t)NH * NTT * 4;
constexpr size_t WS_QST = WS_VST + (size_t)16 * NTT * 4;
constexpr size_t WS_G = WS_QST + (size_t)NH * NTT * 4;
constexpr size_t WS_FQ = WS_G + (size_t)SEQ * 4;
constexpr size_t WS_FA = WS_FQ + 16 * 256;
constexpr size_t WS_FO = WS_FA + 16 * 256;
constexpr size_t WS_EP = WS_FO + 8 * 256;
constexpr size_t WS_SCR = WS_EP + 256;

DI unsigned cvtpk(float lo, float hi) { f32x2 v = {lo, hi}; return __builtin_bit_cast(unsigned, __builtin_convertvector(v, bf16x2_t)); }
DI float bf2f(u16 v) { return __uint_as_float((unsigned)v << 16); }
DI float bflo(unsigned v) { return __uint_as_float(v << 16); }
DI float bfhi(unsigned v) { return __uint_as_float(v & 0xffff0000u); }
DI void glds16(const void* sbase, unsigned voff, unsigned lds_base) {
    unsigned sv;
    asm volatile("s_mov_b32 %0, m0\n\ts_mov_b32 m0, %3\n\ts_nop 0\n\tglobal_load_lds_dwordx4 %1, %2\n\ts_mov_b32 m0, %0"
                 : "=&s"(sv) : "v"(voff), "s"(sbase), "s"(lds_base) : "memory");
}
template <int OFF> DI void gload16(bf16x8& dst, const void* sbase, unsigned voff) {
    asm volatile("global_load_dwordx4 %0, %1, %2 offset:%3" : "=v"(dst) : "v"(voff), "s"(sbase), "i"(OFF) : "memory");
}
DI s16x4 trread(lds_cptr p) { return __builtin_bit_cast(s16x4, __builtin_amdgcn_ds_read_tr16_b64_v4i16((__attribute__((address_space(3))) v4i16_t*)p)); }
DI bf16x8 ldsfrag(lds_cptr p) { return *(const __attribute__((address_space(3))) bf16x8*)p; }
DI bf16x8 cat8(s16x4 a, s16x4 b) { return __builtin_shufflevector(a, b, 0, 1, 2, 3, 4, 5, 6, 7); }
template <int N> DI void wait_vm_bar() { asm volatile("s_waitcnt vmcnt(%0)\n\ts_barrier" :: "i"(N) : "memory"); }
template <int N> DI void wait_vm_lgkm_bar() { asm volatile("s_waitcnt vmcnt(%0) lgkmcnt(0)\n\ts_barrier" :: "i"(N) : "memory"); }
DI float xor32f(float v) {
    auto r = __builtin_amdgcn_permlane32_swap(__float_as_uint(v), __float_as_uint(v), false, false);
    return (threadIdx.x & 32) ? __uint_as_float(r[0]) : __uint_as_float(r[1]);
}
DI float dpp_max63(float v) {
#define DSTEP(ctrl, rmask) v = fmaxf(v, __int_as_float(__builtin_amdgcn_update_dpp(__float_as_int(v), __float_as_int(v), ctrl, rmask, 0xf, false)))
    DSTEP(0x111, 0xf); DSTEP(0x112, 0xf); DSTEP(0x114, 0xf); DSTEP(0x118, 0xf); DSTEP(0x142, 0xa); DSTEP(0x143, 0xc);
#undef DSTEP
    return v;
}
DI float dpp_sum63(float v) {
#define DSTEP(ctrl, rmask) v += __int_as_float(__builtin_amdgcn_update_dpp(0, __float_as_int(v), ctrl, rmask, 0xf, false))
    DSTEP(0x111, 0xf); DSTEP(0x112, 0xf); DSTEP(0x114, 0xf); DSTEP(0x118, 0xf); DSTEP(0x142, 0xa); DSTEP(0x143, 0xc);
#undef DSTEP
    return v;
}
DI float bcast63(float v) { return __int_as_float(__builtin_amdgcn_readlane(__float_as_int(v), 63)); }
DI float mish_f(float h) {
    float t = __builtin_amdgcn_exp2f(-fabsf(h) * 1.4426950408889634f);
    float t2 = t * t;
    float num = h > 0.f ? 1.f + 2.f * t : t2 + 2.f * t;
    float den = h > 0.f ? num + 2.f * t2 : num + 2.f;
    return h * num * __builtin_amdgcn_rcpf(den);
}

typedef __attribute__((address_space(1))) unsigned gu32;
#define RLX_AGENT __ATOMIC_RELAXED, __HIP_MEMORY_SCOPE_AGENT
DI void st16_sc1(void* sbase, unsigned voff, u32x4 v) {
    asm volatile("global_store_dwordx4 %0, %1, %2 sc1\n\ts_nop 1" :: "v"(voff), "v"(v), "s"(sbase) : "memory");
}
DI void st4_sc1(void* p, float v) { __hip_atomic_store((gu32*)p, __float_as_uint(v), RLX_AGENT); }
DI void publish(void* flag, unsigned tag) {
    asm volatile("s_waitcnt vmcnt(0) lgkmcnt(0)\n\ts_barrier" ::: "memory");
    if (threadIdx.x == 0) __hip_atomic_store((gu32*)flag, tag, RLX_AGENT);
}
DI void wait_flags(const void* flags, int n, unsigned tag, int lane) {
    for (unsigned spins = 0; spins < (1u << 18); ++spins) {
        unsigned v = tag;
        if (lane < n) v = __hip_atomic_load((const gu32*)flags + lane, RLX_AGENT);
        if (__all(v == tag)) break;
        __builtin_amdgcn_s_sleep(1);
    }
    __builtin_amdgcn_fence(__ATOMIC_ACQUIRE, "agent");
    asm volatile("s_waitcnt vmcnt(0)" ::: "memory");
}

constexpr int P_BN = 96, P_ROW = P_BN * 2;
constexpr int G_XS = 16384, G_STG = G_XS + 64 * P_ROW, G_NSL = 5, LDS_BYTES = 163840;
constexpr int A_CH = 32768, A_LC = 4 * A_CH, A_LQ = A_LC + 4096;
constexpr int A_CHAIN = A_LQ, L_CTL = A_CHAIN + 20480;
constexpr int C_FAST = L_CTL, C_OACQ = L_CTL + 4, C_OCNT = L_CTL + 8, C_WOR = L_CTL + 12, C_CHAIN = L_CTL + 16, C_FULL = L_CTL + 32, C_FREE = L_CTL + 48;
constexpr int M_BASE = A_CH, M_STG = 16384, M_NSL = 4, M_NS1 = (FF + 63) / 64;
constexpr float A_THR = 40.f;
static_assert(L_CTL >= G_NSL * G_STG && L_CTL + 64 <= LDS_BYTES - 256, "lds map");
typedef __attribute__((address_space(3))) unsigned lu32;
#define RLX_WG __ATOMIC_RELAXED, __HIP_MEMORY_SCOPE_WORKGROUP

DI bf16x8 pack8(const f32x16& p, int s) {
    u32x4 r;
#pragma unroll
    for (int q = 0; q < 4; ++q) r[q] = cvtpk(p[8 * s + 2 * q], p[8 * s + 2 * q + 1]);
    return __builtin_bit_cast(bf16x8, r);
}
#define LGKM_BAR() asm volatile("s_waitcnt lgkmcnt(0)\n\ts_barrier" ::: "memory")
#define BAR() asm volatile("s_barrier" ::: "memory")
template <int N> DI void wait_vm() { asm volatile("s_waitcnt vmcnt(%0)" :: "i"(N) : "memory"); }
DI void raise_flag(void* flag, unsigned tag) { if (threadIdx.x == 0) __hip_atomic_store((gu32*)flag, tag, RLX_AGENT); }
DI void lds_wait_ge(char* lds, int off, unsigned want) {
    asm volatile("" ::: "memory");
    while ((unsigned)__builtin_amdgcn_readfirstlane(__hip_atomic_load((lu32*)((lds_ptr)lds + off), RLX_WG)) < want) __builtin_amdgcn_s_sleep(1);
    asm volatile("" ::: "memory");
}
DI void lds_wait4_ge(char* lds, int off, unsigned want) {
    asm volatile("" ::: "memory");
    for (;;) {
        const u32x4 v = *(volatile __attribute__((address_space(3))) u32x4*)((lds_ptr)lds + off);
        const unsigned m = min(min(v[0], v[1]), min(v[2], v[3]));
        if ((unsigned)__builtin_amdgcn_readfirstlane(m) >= want) break;
        __builtin_amdgcn_s_sleep(0);
    }
    asm volatile("" ::: "memory");
}
DI void lds_add1(char* lds, int off, int lane) {
    asm volatile("s_waitcnt lgkmcnt(0)" ::: "memory");
    if (lane == 0) __hip_atomic_fetch_add((lu32*)((lds_ptr)lds + off), 1u, RLX_WG);
}
DI bool try_flags(const void* flags, int n, unsigned tag, int lane) {
    unsigned v = tag;
    if (lane < n) v = __hip_atomic_load((const gu32*)flags + lane, RLX_AGENT);
    if (!__all(v == tag)) return false;
    __builtin_amdgcn_fence(__ATOMIC_ACQUIRE, "agent");
    return true;
}

DI void l2_touch(const void* sbase, unsigned voff, unsigned lds_scratch) {
    unsigned sv;
    asm volatile("s_mov_b32 %0, m0\n\ts_mov_b32 m0, %3\n\ts_nop 0\n\tglobal_load_lds_dword %1, %2\n\ts_mov_b32 m0, %0"
                 : "=&s"(sv) : "v"(voff), "s"(sbase), "s"(lds_scratch) : "memory");
}
DI void l2_touch64(const void* p, unsigned lds_scratch) {
    unsigned sv;
    asm volatile("s_mov_b32 %0, m0\n\ts_mov_b32 m0, %2\n\ts_nop 0\n\tglobal_load_lds_dword %1, off\n\ts_mov_b32 m0, %0"
                 : "=&s"(sv) : "v"(p), "s"(lds_scratch) : "memory");
}
constexpr int L_SCR = 163840 - 256;
struct GFrag { bf16x8 x[4]; bf16x8 w[4]; };
DI void g_rd(GFrag& f, lds_cptr b, const int (&xa)[4], int wa) {
#pragma unroll
    for (int kk = 0; kk < 4; ++kk) {
        f.x[kk] = ldsfrag(b + xa[kk]);
        f.w[kk] = cat8(trread(b + wa + kk * 16 * P_ROW), trread(b + wa + (kk * 16 + 4) * P_ROW));
    }
}
template <bool ISV> DI void g_mma(f32x16& acc, const GFrag& f) {
    SBAR();
#pragma unroll
    for (int kk = 0; kk < 4; ++kk) { if (ISV) acc = MFMA(f.x[kk], f.w[kk], acc); else acc = MFMA(f.w[kk], f.x[kk], acc); }
    PIN(acc);
    SBAR();
}
struct MFrag4 { bf16x8 a[4], w[4]; };
DI void m_rd4(MFrag4& f, lds_cptr A, lds_cptr W) {
#pragma unroll
    for (int kk = 0; kk < 4; ++kk) { f.w[kk] = cat8(trread(W + kk * 2048), trread(W + kk * 2048 + 1024)); f.a[kk] = ldsfrag(A + kk * 1024); }
}

__global__ __launch_bounds__(1024, 4) void fused_kernel(const u16* __restrict__ x, const u16* __restrict__ wqkv, const u16* __restrict__ wo,
                                                        const u16* __restrict__ w1, const u16* __restrict__ b1, const u16* __restrict__ w2,
                                                        const u16* __restrict__ gate_w, char* __restrict__ ws, u16* __restrict__ out) {
    extern __shared__ __attribute__((aligned(16))) char lds[];
    const int tid = threadIdx.x, lane = tid & 63, wid = __builtin_amdgcn_readfirstlane(tid >> 6);
    const int r = lane & 31, hi = lane >> 5;
    const int xg = blockIdx.x & 7, idx = blockIdx.x >> 3;
    const unsigned lds0 = (unsigned)(uintptr_t)lds;
    const unsigned l16 = lane * 16;
    const int i16 = lane & 15, g4 = lane >> 4;
    TS(0);
    const int mt = (xg >> 1) * 4 + (idx & 3), nt = (xg & 1) * 8 + (idx >> 2);
    const int head = xg * 4 + (idx & 3), qg = idx >> 2;
    const int mt4 = xg * 4 + (idx & 3), nt4 = idx >> 2;
    const int m0 = mt * 128;
    unsigned tag;

    if (wid >= 12) {
        const int lw = wid - 12;
        __builtin_amdgcn_s_setprio(3);
        const int rot = ((idx >> 2) + 2 * (idx & 3)) & 7;
        unsigned epv;
        asm volatile("global_load_dword %0, %1, %2 sc1" : "=v"(epv) : "v"(0u), "s"(ws + WS_EP) : "memory");
        unsigned woq[3], wof[3];
#pragma unroll
        for (int j = 0; j < 3; ++j) {
            const int o = (lw * 3 + j) * 1024 + lane * 16;
            const int k = o / P_ROW, cc = (o % P_ROW) >> 4;
            woq[j] = (unsigned)(k * NQKV + (cc >> 2) * 512 + nt * 32 + (cc & 3) * 8) * 2u;
            int n = nt * P_BN + cc * 8;
            n = n > FF - 8 ? FF - 8 : n;
            wof[j] = (unsigned)(k * FF + n) * 2u;
        }
        const unsigned xoff = (unsigned)((m0 + lw * 32 + (lane >> 3)) * DM) * 2u + (((lane & 7) ^ g4) << 4), xoff1 = xoff ^ 64u;
        auto issue = [&](int s, int slot) {
            const int kb = (s + rot) & 7;
            const unsigned base = lds0 + slot * G_STG;
            const u16* xs = x + kb * 64;
            glds16(xs, xoff, base + lw * 4096);
            glds16(xs + 8 * DM, xoff1, base + lw * 4096 + 1024);
            glds16(xs + 16 * DM, xoff, base + lw * 4096 + 2048);
            glds16(xs + 24 * DM, xoff1, base + lw * 4096 + 3072);
            const u16* wsrc = s < 8 ? wqkv + (size_t)kb * 64 * NQKV : w1 + (size_t)kb * 64 * FF;
#pragma unroll
            for (int j = 0; j < 3; ++j) glds16(wsrc, s < 8 ? woq[j] : wof[j], base + G_XS + (lw * 3 + j) * 1024);
        };
        issue(0, 0); issue(1, 1);
        asm volatile("s_waitcnt vmcnt(7)\n\ts_barrier" : "+v"(epv) :: "memory");
        tag = (unsigned)__builtin_amdgcn_readfirstlane(epv) * 0x9E3779B1u + 0x7F4A7C15u;
        issue(2, 2); issue(3, 3);
        wait_vm<14>(); BAR();
        issue(4, 4); issue(5, 0);
        {
            const int ln = idx * 192 + (lw < 3 ? lw * 64 + lane : 0), row = ln / 12;
            int col = (xg & 1) * 768 + (ln - row * 12) * 64;
            col = col > FF - 2 ? FF - 2 : col;
            l2_touch(w1, (unsigned)(row * FF + col) * 2u, lds0 + L_SCR);
        }
        wait_vm<22>(); BAR(); issue(6, 1);
        wait_vm<22>(); BAR(); issue(7, 2);
        wait_vm<22>(); BAR(); issue(8, 3);
        wait_vm<22>(); BAR(); issue(9, 4);
        int slot = 0;
#pragma unroll 1
        for (int s = 5; s < 11; ++s) {
            wait_vm<21>(); BAR();
            issue(s + G_NSL, slot);
            slot = slot + 1 == G_NSL ? 0 : slot + 1;
        }
        wait_vm<21>(); BAR();
        if (lw == 3) {
            const void* fq = ws + WS_FQ + (head >> 1) * 256;
            bool ok = try_flags(fq, 16, tag, lane);
            BAR();
            if (!ok) ok = try_flags(fq, 16, tag, lane);
            BAR();
            if (!ok) wait_flags(fq, 16, tag, lane);
            wait_vm<0>(); BAR();
        } else {
            wait_vm<14>(); BAR();
            wait_vm<7>(); BAR();
            wait_vm<0>(); BAR();
        }
        float kst, vst, qst;
        asm volatile("global_load_dword %0, %1, %2" : "=v"(kst) : "v"((unsigned)lane * 4u), "s"(ws + WS_KST + head * 256) : "memory");
        asm volatile("global_load_dword %0, %1, %2" : "=v"(vst) : "v"((unsigned)lane * 4u), "s"(ws + WS_VST + (head >> 1) * 256) : "memory");
        asm volatile("global_load_dword %0, %1, %2" : "=v"(qst) : "v"((unsigned)(lane & 7) * 4u), "s"(ws + WS_QST + head * 256 + qg * 32) : "memory");
        glds16(ws + WS_Q + (size_t)(head * NTT + qg * 8 + lw * 2) * 1024, l16, lds0 + A_LQ + lw * 2048);
        glds16(ws + WS_Q + (size_t)(head * NTT + qg * 8 + lw * 2 + 1) * 1024, l16, lds0 + A_LQ + lw * 2048 + 1024);
#pragma unroll
        for (int t = 0; t < 8; ++t) {
            glds16(ws + WS_K + (size_t)(head * NTT + lw * 16 + t) * 1024, l16, lds0 + ((t >> 2) + 1) * A_CH + lw * 8192 + (t & 3) * 1024);
            glds16(ws + WS_V + (size_t)(head * NTT + lw * 16 + t) * 1024, l16, lds0 + ((t >> 2) + 1) * A_CH + lw * 8192 + 4096 + (t & 3) * 1024);
        }
        asm volatile("s_waitcnt vmcnt(18)" : "+v"(kst), "+v"(vst), "+v"(qst) :: "memory");
        if (lw == 0) {
            const float kmax = dpp_max63(kst), vmax = dpp_max63(vst), qmax = dpp_max63(qst);
            if (lane == 63) *(lu32*)((lds_ptr)lds + C_FAST) = (qmax * kmax * 1.01f <= A_THR * A_THR && vmax <= 1e18f) ? 1u : 0u;
        }
        wait_vm<8>();
        __builtin_amdgcn_s_setprio(0);
    } else {
        const int tw = wid & 3, ns = wid >> 2;
        if (tid < 16) *(lu32*)((lds_ptr)lds + L_CTL + tid * 4) = 0u;
        tag = (unsigned)__builtin_amdgcn_readfirstlane(__hip_atomic_load((const gu32*)(ws + WS_EP), RLX_AGENT)) * 0x9E3779B1u + 0x7F4A7C15u;
        const int gtok = m0 + nt * 8 + (wid & 7);
        const u32x4 gx = *(const u32x4*)(x + (size_t)gtok * DM + lane * 8);
        const u32x4 gw = *(const u32x4*)(gate_w + lane * 8);
        float gs = 0.f;
#pragma unroll
        for (int q = 0; q < 4; ++q) { gs = fmaf(bflo(gx[q]), bflo(gw[q]), gs); gs = fmaf(bfhi(gx[q]), bfhi(gw[q]), gs); }
        gs = dpp_sum63(gs);
        PIN(gs);
        const int xrow = tw * 32 + r, xf7 = (xrow >> 1) & 7;
        int xa[4];
#pragma unroll
        for (int kk = 0; kk < 4; ++kk) xa[kk] = xrow * 128 + (((2 * kk + hi) ^ xf7) << 4);
        const int wa = G_XS + ((g4 >> 1) * 8 + (i16 >> 2)) * P_ROW + (ns * 32 + (g4 & 1) * 16 + (i16 & 3) * 4) * 2;
        f32x16 acc = f32x16{};
        GFrag fa, fb;
        LGKM_BAR();
        g_rd(fa, (lds_cptr)lds, xa, wa);
        TS(1);
        int so = G_STG;
#define GSTEP(FC, FN, ISV) { LGKM_BAR(); g_rd(FN, (lds_cptr)lds + so, xa, wa); so = so + G_STG == G_NSL * G_STG ? 0 : so + G_STG; g_mma<ISV>(acc, FC); }
        if (ns == 2) {
#pragma unroll 1
            for (int s = 0; s < 8; s += 2) { GSTEP(fa, fb, true) GSTEP(fb, fa, true) }
        } else {
#pragma unroll 1
            for (int s = 0; s < 8; s += 2) { GSTEP(fa, fb, false) GSTEP(fb, fa, false) }
        }
        TS(2);
        const int tt = mt * 4 + tw, h0 = nt * 2;
        if (ns < 2) {
            const float sc = ns == 0 ? QSCALE : 1.f;
            float ss[2];
#pragma unroll
            for (int e = 0; e < 2; ++e) {
                u32x4 p; float s2 = 0.f;
#pragma unroll
                for (int q = 0; q < 4; ++q) {
                    p[q] = cvtpk(acc[8 * e + 2 * q] * sc, acc[8 * e + 2 * q + 1] * sc);
                    const float a = bflo(p[q]), b = bfhi(p[q]);
                    s2 = fmaf(a, a, s2); s2 = fmaf(b, b, s2);
                }
                st16_sc1(ws + (ns == 0 ? WS_Q : WS_K) + (size_t)((h0 + e) * NTT + tt) * 1024, l16, p);
                ss[e] = s2;
            }
#pragma unroll
            for (int e = 0; e < 2; ++e) {
                ss[e] += xor32f(ss[e]);
                ss[e] = (ss[e] == ss[e]) ? ss[e] : __builtin_inff();
                ss[e] = dpp_max63(ss[e]);
            }
            if (lane == 63) {
                st4_sc1(ws + (ns == 0 ? WS_QST : WS_KST) + (size_t)(h0 * NTT + tt) * 4, ss[0]);
                st4_sc1(ws + (ns == 0 ? WS_QST : WS_KST) + (size_t)((h0 + 1) * NTT + tt) * 4, ss[1]);
            }
        } else {
            const unsigned vo = (unsigned)((r >> 4) * NTT * 1024 + hi * 256 + (r & 15) * 16);
            float vm = 0.f;
#pragma unroll
            for (int s2 = 0; s2 < 2; ++s2) {
                u32x4 pv;
#pragma unroll
                for (int q = 0; q < 4; ++q) {
                    const float a = acc[8 * s2 + 2 * q], b = acc[8 * s2 + 2 * q + 1];
                    pv[q] = cvtpk(a, b);
                    vm = fmaxf(vm, fmaxf((a == a) ? fabsf(a) : __builtin_inff(), (b == b) ? fabsf(b) : __builtin_inff()));
                }
                st16_sc1(ws + WS_V + (size_t)(h0 * NTT + tt) * 1024 + s2 * 512, vo, pv);
            }
            vm = dpp_max63(vm);
            if (lane == 63) st4_sc1(ws + WS_VST + (size_t)(nt * NTT + tt) * 4, vm);
        }
        if (wid < 8 && lane == 63) st4_sc1(ws + WS_G + (size_t)gtok * 4, 1.f / (1.f + __expf(-gs)));
        TS(3);
        acc = f32x16{};
        GSTEP(fa, fb, false)
        wait_vm<0>();
        GSTEP(fb, fa, false)
        raise_flag(ws + WS_FQ + nt * 256 + mt * 4, tag);
        GSTEP(fa, fb, false) GSTEP(fb, fa, false) GSTEP(fa, fb, false) GSTEP(fb, fa, false)
        u32x2 bia[2], bib[2];
        {
            int hi2 = tid; asm volatile("" : "+v"(hi2)); hi2 = (hi2 >> 5) & 1;
#pragma unroll
            for (int e = 0; e < 2; ++e) {
                int f = nt * P_BN + ns * 32 + 16 * e + 4 * hi2;
                f = f < FF ? f : 0;
                bia[e] = *(const u32x2*)(b1 + f);
                bib[e] = *(const u32x2*)(b1 + f + 8);
            }
        }
        GSTEP(fa, fb, false)
#undef GSTEP
        g_mma<false>(acc, fb);
        TS(4);
#pragma unroll
        for (int e = 0; e < 2; ++e) {
            const int f0 = nt * P_BN + ns * 32 + 16 * e;
            const u32x2 ba = bia[e], bb = bib[e];
            u32x4 p;
            p[0] = cvtpk(mish_f(acc[8 * e + 0] + bflo(ba[0])), mish_f(acc[8 * e + 1] + bfhi(ba[0])));
            p[1] = cvtpk(mish_f(acc[8 * e + 2] + bflo(ba[1])), mish_f(acc[8 * e + 3] + bfhi(ba[1])));
            p[2] = cvtpk(mish_f(acc[8 * e + 4] + bflo(bb[0])), mish_f(acc[8 * e + 5] + bfhi(bb[0])));
            p[3] = cvtpk(mish_f(acc[8 * e + 6] + bflo(bb[1])), mish_f(acc[8 * e + 7] + bfhi(bb[1])));
            if (f0 < FF) st16_sc1(ws + WS_ACT + (size_t)(tt * FKS + (f0 >> 4)) * 1024, l16, p);
        }
        if (tid < 256) {
            const unsigned one2 = ((tid & 15) == 0 || (tid & 15) == 4) ? 0x3f803f80u : 0u;
            *(__attribute__((address_space(3))) u32x4*)((lds_ptr)lds + A_LC + tid * 16) = u32x4{one2, one2, one2, one2};
        }
    }
    LGKM_BAR();
    TS(5);
    const int qp = wid & 3, kq = wid >> 2;
    {
        const bf16x8 qa = ldsfrag((lds_cptr)lds + A_LQ + qp * 2048 + lane * 16), qb = ldsfrag((lds_cptr)lds + A_LQ + qp * 2048 + 1024 + lane * 16);
        const bool fast = __builtin_amdgcn_readfirstlane(*(const lu32*)((lds_cptr)lds + C_FAST)) != 0;
        f32x16 oa = f32x16{}, ob = f32x16{};
        float ma = 0.f, mb = 0.f;
        const int kbase = kq * 8192 + lane * 16;
        const int vbase = r < 16 ? kq * 8192 + 4096 + hi * 256 + r * 16 : A_LC + hi * 256 + (r - 16) * 16;
        const int vmul = r < 16 ? A_CH : 0;
        const f32x16 zero = f32x16{};
        auto chunk_mid = [&](int c) {
            if (c == 0) {
                if (kq == 0) {
#pragma unroll
                    for (int t = 8; t < 16; ++t) {
                        glds16(ws + WS_K + (size_t)(head * NTT + qp * 16 + t) * 1024, l16, lds0 + (((t >> 2) + 1) & 3) * A_CH + qp * 8192 + (t & 3) * 1024);
                        glds16(ws + WS_V + (size_t)(head * NTT + qp * 16 + t) * 1024, l16, lds0 + (((t >> 2) + 1) & 3) * A_CH + qp * 8192 + 4096 + (t & 3) * 1024);
                    }
#pragma unroll
                    for (int j = 0; j < 2; ++j) {
                        const int o = (qp * 2 + j) * 64 + lane;
                        const int ln = idx * 472 + (o < 472 ? o : 0);
                        l2_touch64(ln < 11008 ? (const char*)w2 + (size_t)ln * 128 : (const char*)wo + (size_t)(ln - 11008) * 128, lds0 + L_SCR);
                    }
                }
                wait_vm<0>(); BAR();
                if (nt < 15) raise_flag(ws + WS_FA + mt * 256 + nt * 4, tag);
            } else if (c == 2) {
                if (wid == 0) {
                    wait_flags(ws + WS_FA + (mt4 >> 1) * 256, 15, tag, lane);
#pragma unroll
                    for (int j = 0; j < 3; ++j) {
                        const int o = j * 64 + lane;
                        l2_touch(ws + WS_ACT + (size_t)mt4 * 2 * FKS * 1024, (unsigned)(nt4 * 172 + (o < 172 ? o : 0)) * 128u, lds0 + L_SCR);
                    }
                }
                BAR();
            }
        };
        if (fast) {
            lds_cptr kc = (lds_cptr)lds + kbase + A_CH, vc = (lds_cptr)lds + vbase + vmul;
            bf16x8 kf = ldsfrag(kc);
            bf16x8 va0 = ldsfrag(vc), va1 = ldsfrag(vc + 512), vb0, vb1;
            f32x16 sa = MFMA(kf, qa, zero), sb = MFMA(kf, qb, zero);
            kf = ldsfrag(kc + 1024);
#define HALF(S, O, V0, V1)                                                              \
                _Pragma("unroll")                                                       \
                for (int i = 0; i < 8; ++i) S[i] = __builtin_amdgcn_exp2f(S[i]);        \
                O = MFMA(V0, pack8(S, 0), O);                                           \
                PIN(O); SBAR();                                                         \
                _Pragma("unroll")                                                       \
                for (int i = 8; i < 16; ++i) S[i] = __builtin_amdgcn_exp2f(S[i]);       \
                O = MFMA(V1, pack8(S, 1), O);                                           \
                PIN(O); SBAR();
#define TILE(VC0, VC1, VN0, VN1, KNEXT, VNEXT)                                          \
            {                                                                           \
                VN0 = ldsfrag(VNEXT); VN1 = ldsfrag((VNEXT) + 512);                     \
                SBAR();                                                                 \
                HALF(sa, oa, VC0, VC1)                                                  \
                sa = MFMA(kf, qa, zero); PIN(sa); SBAR();                               \
                HALF(sb, ob, VC0, VC1)                                                  \
                sb = MFMA(kf, qb, zero); PIN(sb);                                       \
                kf = ldsfrag(KNEXT);                                                    \
                SBAR();                                                                 \
            }
#pragma unroll 1
            for (int c = 0; c < 4; ++c) {
                const int nr = c < 3 ? (c + 2) & 3 : 0;
                lds_cptr kn = (lds_cptr)lds + kbase + nr * A_CH, vn = (lds_cptr)lds + vbase + nr * vmul;
                TILE(va0, va1, vb0, vb1, kc + 2048, vc + 1024)
                TILE(vb0, vb1, va0, va1, kc + 3072, vc + 2048)
                chunk_mid(c);
                TILE(va0, va1, vb0, vb1, kn, vc + 3072)
                TILE(vb0, vb1, va0, va1, kn + 1024, vn)
                kc = kn; vc = vn;
            }
#undef TILE
#undef HALF
        } else {
            ma = -__builtin_inff(); mb = ma;
#pragma unroll 1
            for (int c = 0; c < 4; ++c) {
                lds_cptr kc = (lds_cptr)lds + kbase + ((c + 1) & 3) * A_CH, vc = (lds_cptr)lds + vbase + ((c + 1) & 3) * vmul;
#pragma unroll 1
                for (int t = 0; t < 4; ++t) {
                    if (t == 2) chunk_mid(c);
                    const bf16x8 kf = ldsfrag(kc + t * 1024);
                    const bf16x8 v0 = ldsfrag(vc + t * 1024), v1 = ldsfrag(vc + t * 1024 + 512);
#define SLOW(Q, O, M)                                                                   \
                    {                                                                   \
                        f32x16 s = MFMA(kf, Q, zero);                                   \
                        float mx = s[0];                                                \
                        _Pragma("unroll")                                               \
                        for (int i = 1; i < 16; ++i) mx = fmaxf(mx, s[i]);              \
                        mx = fmaxf(mx, xor32f(mx));                                     \
                        const float mn = fmaxf(M, mx);                                  \
                        const float al = __builtin_amdgcn_exp2f(M - mn);                \
                        M = mn;                                                         \
                        _Pragma("unroll")                                               \
                        for (int i = 0; i < 16; ++i) { s[i] = __builtin_amdgcn_exp2f(s[i] - mn); O[i] *= al; } \
                        O = MFMA(v0, pack8(s, 0), O);                                   \
                        O = MFMA(v1, pack8(s, 1), O);                                   \
                    }
                    SLOW(qa, oa, ma)
                    SLOW(qb, ob, mb)
#undef SLOW
                }
            }
        }
        TS(6);
        {
            int tz = tid; asm volatile("" : "+v"(tz));
            lds_ptr ch = (lds_ptr)lds + A_CHAIN + qp * 5120 + (tz & 63) * 16;
            lds_ptr cl = (lds_ptr)lds + A_CHAIN + qp * 5120 + 4096 + (tz & 63) * 16;
            if (kq > 0) {
                lds_wait_ge(lds, C_CHAIN + qp * 4, (unsigned)kq);
                const f32x4 a0 = *(__attribute__((address_space(3))) f32x4*)(ch), a1 = *(__attribute__((address_space(3))) f32x4*)(ch + 1024);
                const f32x4 b0 = *(__attribute__((address_space(3))) f32x4*)(ch + 2048), b1v = *(__attribute__((address_space(3))) f32x4*)(ch + 3072);
                const f32x4 lm = *(__attribute__((address_space(3))) f32x4*)(cl);
                const float na = fmaxf(ma, lm[1]), nb = fmaxf(mb, lm[3]);
                const float fa = __builtin_amdgcn_exp2f(ma - na), xa2 = __builtin_amdgcn_exp2f(lm[1] - na);
                const float fb = __builtin_amdgcn_exp2f(mb - nb), xb2 = __builtin_amdgcn_exp2f(lm[3] - nb);
#pragma unroll
                for (int e = 0; e < 4; ++e) {
                    oa[e] = fmaf(a0[e], xa2, oa[e] * fa); oa[4 + e] = fmaf(a1[e], xa2, oa[4 + e] * fa);
                    ob[e] = fmaf(b0[e], xb2, ob[e] * fb); ob[4 + e] = fmaf(b1v[e], xb2, ob[4 + e] * fb);
                }
                oa[8] = fmaf(lm[0], xa2, oa[8] * fa); ob[8] = fmaf(lm[2], xb2, ob[8] * fb);
                ma = na; mb = nb;
            }
            if (kq < 3) {
                *(__attribute__((address_space(3))) f32x4*)(ch) = f32x4{oa[0], oa[1], oa[2], oa[3]};
                *(__attribute__((address_space(3))) f32x4*)(ch + 1024) = f32x4{oa[4], oa[5], oa[6], oa[7]};
                *(__attribute__((address_space(3))) f32x4*)(ch + 2048) = f32x4{ob[0], ob[1], ob[2], ob[3]};
                *(__attribute__((address_space(3))) f32x4*)(ch + 3072) = f32x4{ob[4], ob[5], ob[6], ob[7]};
                *(__attribute__((address_space(3))) f32x4*)(cl) = f32x4{oa[8], ma, ob[8], mb};
                lds_add1(lds, C_CHAIN + qp * 4, lane);
            } else {
                const float ra = 1.f / oa[8], rb = 1.f / ob[8];
                u32x4 pa, pb;
#pragma unroll
                for (int q = 0; q < 4; ++q) { pa[q] = cvtpk(oa[2 * q] * ra, oa[2 * q + 1] * ra); pb[q] = cvtpk(ob[2 * q] * rb, ob[2 * q + 1] * rb); }
                const int qt = qg * 8 + qp * 2;
                st16_sc1(ws + WS_O + (size_t)(qt * NH + head) * 1024, l16, pa);
                st16_sc1(ws + WS_O + (size_t)((qt + 1) * NH + head) * 1024, l16, pb);
                wait_vm<0>();
                unsigned prev = 0;
                if (lane == 0) prev = __hip_atomic_fetch_add((lu32*)((lds_ptr)lds + C_OCNT), 1u, RLX_WG);
                if (__builtin_amdgcn_readfirstlane(prev) == 3) {
                    if (lane == 0) __hip_atomic_store((gu32*)(ws + WS_FO + qg * 256 + head * 4), tag, RLX_AGENT);
                    const unsigned wo_l = (unsigned)((((tz >> 5) & 1) * 4 + ((tz >> 2) & 3)) * DM + nt4 * 64 + ((tz >> 4) & 1) * 32 + (tz & 3) * 8) * 2u;
#pragma unroll 1
                    for (int pc = 0; pc < 64; pc += 4) {
                        const unsigned dst = lds0 + (pc < 32 ? 3 * A_CH + pc * 1024 : (pc - 32) * 1024);
#pragma unroll
                        for (int j = 0; j < 4; ++j) glds16(wo + (size_t)(pc + j) * 8 * DM, wo_l, dst + j * 1024);
                    }
                    wait_vm<0>();
                    if (lane == 0) *(volatile lu32*)((lds_ptr)lds + C_WOR) = 1u;
                }
            }
        }
        TS(7);
    }
    if (kq >= 2) return;
    {
        int tz = tid; asm volatile("" : "+v"(tz));
        const unsigned lz16 = (tz & 63) * 16;
        if (kq == 0) {
            const char* aF = ws + WS_ACT + (size_t)(mt4 * 2 * FKS + qp) * 1024;
            const unsigned wo_l = (unsigned)((qp * 8 + ((tz >> 5) & 1) * 4 + ((tz >> 2) & 3)) * DM + nt4 * 64 + ((tz >> 4) & 1) * 32 + (tz & 3) * 8) * 2u;
            const unsigned lb = lds0 + M_BASE + qp * 1024;
            auto issue = [&](const char* a, const u16* w, int wadj, int s) {
                const unsigned base = lb + (s & 3) * M_STG;
                glds16(a, lz16, base);
                glds16(a + (size_t)FKS * 1024, lz16, base + 4096);
                glds16(w, wo_l, base + 8192);
                glds16(w + wadj * DM, wo_l, base + 12288);
            };
            auto publish_full = [&](int n) { if (lane == 0) *(volatile lu32*)((lds_ptr)lds + C_FULL + qp * 4) = (unsigned)n; };
#pragma unroll
            for (int s = 0; s < 4; ++s) issue(aF + (size_t)s * 4096, w2 + (size_t)s * 64 * DM, 32, s);
            wait_vm<0>(); publish_full(4);
            TS(8);
#pragma unroll 1
            for (int s = 4; s < M_NS1; ++s) {
                wait_vm<8>(); publish_full(s - 2 > 4 ? s - 2 : 4);
                lds_wait4_ge(lds, C_FREE, (unsigned)(s - 3));
                const bool lastst = s == M_NS1 - 1;
                issue(aF + (size_t)s * 4096 - ((lastst && (qp & 2)) ? 2048 : 0), w2 + (size_t)s * 64 * DM, lastst ? 0 : 32, s);
            }
            wait_vm<8>(); publish_full(M_NS1 - 2);
            wait_vm<4>(); publish_full(M_NS1 - 1);
            wait_vm<0>(); publish_full(M_NS1);
            TS(11);
            if (qp == 0) {
                wait_flags(ws + WS_FO + (mt4 >> 2) * 256, 32, tag, lane);
                if (lane == 0) *(volatile lu32*)((lds_ptr)lds + C_OACQ) = 1u;
            } else {
                lds_wait_ge(lds, C_OACQ, 1u);
            }
            const char* aO = ws + WS_O + (size_t)(mt4 * 2 * NH + qp) * 1024;
#pragma unroll
            for (int j = 0; j < 4; ++j) {
                const int s = M_NS1 + j;
                lds_wait4_ge(lds, C_FREE, (unsigned)(s - 3));
                const unsigned base = lb + (s & 3) * M_STG;
                glds16(aO + (size_t)j * 8192, lz16, base);
                glds16(aO + (size_t)j * 8192 + 4096, lz16, base + 4096);
                glds16(aO + (size_t)(NH + j * 8) * 1024, lz16, base + 8192);
                glds16(aO + (size_t)(NH + j * 8) * 1024 + 4096, lz16, base + 12288);
            }
            wait_vm<12>(); publish_full(M_NS1 + 1);
            wait_vm<8>(); publish_full(M_NS1 + 2);
            wait_vm<4>(); publish_full(M_NS1 + 3);
            wait_vm<0>(); publish_full(M_NS1 + 4);
        } else {
            const int wm = qp & 1, wn = qp >> 1;
            const int wl = (((tz >> 5) & 1) * 2 + wn) * 256 + ((tz >> 2) & 3) * 64 + ((tz >> 4) & 1) * 32 + (tz & 3) * 8;
            lds_cptr W = (lds_cptr)lds + M_BASE + 8192 + wl;
            lds_cptr A = (lds_cptr)lds + M_BASE + wm * 4096 + lz16;
            f32x16 acca = f32x16{}, accb = f32x16{};
            MFrag4 fa, fb;
            auto release = [&](int n) {
                asm volatile("s_waitcnt lgkmcnt(0)" ::: "memory");
                if (lane == 0) *(volatile lu32*)((lds_ptr)lds + C_FREE + qp * 4) = (unsigned)n;
            };
            lds_wait4_ge(lds, C_FULL, 1u);
            m_rd4(fa, A, W);
            release(1);
#define MIT(S, FC, FN, NK)                                                                                               \
            {                                                                                                            \
                if ((S) + 1 < M_NS1) {                                                                                   \
                    lds_wait4_ge(lds, C_FULL, (unsigned)((S) + 2));                                                      \
                    m_rd4(FN, A + (((S) + 1) & 3) * M_STG, W + (((S) + 1) & 3) * M_STG);                                 \
                }                                                                                                        \
                SBAR();                                                                                                  \
                _Pragma("unroll")                                                                                        \
                for (int kk = 0; kk < NK; ++kk) accb = MFMA(FC.w[kk], FC.a[kk], accb);                                   \
                PIN(accb);                                                                                               \
                SBAR();                                                                                                  \
                if ((S) + 1 < M_NS1) release((S) + 2);                                                                   \
            }
#pragma unroll 1
            for (int s = 0; s < M_NS1 - 2; s += 2) { MIT(s, fa, fb, 4) MIT(s + 1, fb, fa, 4) }
            MIT(M_NS1 - 2, fa, fb, 4) MIT(M_NS1 - 1, fb, fa, 2)
#undef MIT
            lds_wait_ge(lds, C_WOR, 1u);
            float g = 0.f;
            lds_cptr AO = (lds_cptr)lds + M_BASE + wm * 8192 + lz16;
#pragma unroll
            for (int j = 0; j < 4; ++j) {
                lds_wait4_ge(lds, C_FULL, (unsigned)(M_NS1 + j + 1));
                if (j == 0) asm volatile("global_load_dword %0, %1, %2" : "=v"(g) : "v"((unsigned)(mt4 * 64 + wm * 32 + (tz & 31)) * 4u), "s"(ws + WS_G) : "memory");
                lds_cptr Wj = (lds_cptr)lds + (j < 2 ? 3 * A_CH + j * 16384 : (j - 2) * 16384) + wl;
                m_rd4(fa, AO + ((M_NS1 + j) & 3) * M_STG, Wj);
                m_rd4(fb, AO + ((M_NS1 + j) & 3) * M_STG + 4096, Wj + 8192);
#pragma unroll
                for (int kk = 0; kk < 4; ++kk) acca = MFMA(fa.w[kk], fa.a[kk], acca);
#pragma unroll
                for (int kk = 0; kk < 4; ++kk) acca = MFMA(fb.w[kk], fb.a[kk], acca);
            }
            asm volatile("s_waitcnt vmcnt(0)" : "+v"(g) :: "memory");
            TS(9);
            const float g1 = 1.f - g;
            u16* orow = out + (size_t)(mt4 * 64 + wm * 32 + (tz & 31)) * DM + nt4 * 64 + wn * 32 + ((tz >> 5) & 1) * 4;
#pragma unroll
            for (int q = 0; q < 4; ++q) {
                u32x2 p;
                p[0] = cvtpk(g * acca[4 * q] + g1 * accb[4 * q], g * acca[4 * q + 1] + g1 * accb[4 * q + 1]);
                p[1] = cvtpk(g * acca[4 * q + 2] + g1 * accb[4 * q + 2], g * acca[4 * q + 3] + g1 * accb[4 * q + 3]);
                *(u32x2*)(orow + q * 8) = p;
            }
        }
    }
    if (blockIdx.x == 0 && tid == 0) __hip_atomic_store((gu32*)(ws + WS_EP), tag, RLX_AGENT);
    TS(10);
}

extern "C" void kernel_launch(void* const* d_in, const int* in_sizes, int n_in, void* d_out, int out_size, void* d_ws, size_t ws_size,
                              hipStream_t stream) {
    (void)hipFuncSetAttribute((const void*)fused_kernel, hipFuncAttributeMaxDynamicSharedMemorySize, LDS_BYTES);
    fused_kernel<<<256, 1024, LDS_BYTES, stream>>>((const u16*)d_in[0], (const u16*)d_in[1], (const u16*)d_in[2], (const u16*)d_in[4],
                                                   (const u16*)d_in[5], (const u16*)d_in[6], (const u16*)d_in[7], (char*)d_ws, (u16*)d_out);
}
```

```cpp
#include <hip/hip_runtime.h>
#include <hip/hip_bf16.h>
#include <stdint.h>

using bf16x8 = __attribute__((ext_vector_type(8))) short;
using s16x4  = __attribute__((ext_vector_type(4))) short;
using f32x16 = __attribute__((ext_vector_type(16))) float;
using f32x4  = __attribute__((ext_vector_type(4))) float;
using f32x2  = __attribute__((ext_vector_type(2))) float;
using u32x4  = __attribute__((ext_vector_type(4))) unsigned;
using u32x2  = __attribute__((ext_vector_type(2))) unsigned;
typedef __bf16 bf16x2_t __attribute__((ext_vector_type(2)));
typedef short v4i16_t __attribute__((ext_vector_type(4)));
typedef __attribute__((address_space(3))) const char* lds_cptr;
typedef __attribute__((address_space(3))) char* lds_ptr;
typedef unsigned short u16;

#define DI __device__ __forceinline__
#define TS(i)
#define TSEND(k)
#define MFMA(a, b, c) __builtin_amdgcn_mfma_f32_32x32x16_bf16((a), (b), (c), 0, 0, 0)
#define SBAR() __builtin_amdgcn_sched_barrier(0)
#define PIN(x) asm volatile("" : "+v"(x))

constexpr int SEQ = 2048, DM = 512, NH = 32, DH = 16, FF = 1376, NQKV = 1536;
constexpr int NTT = SEQ / 32;
constexpr int FKS = FF / 16;
constexpr float QSCALE = 0.25f * 1.4426950408889634f;

constexpr size_t WS_Q = 0;
constexpr size_t WS_K = WS_Q + (size_t)SEQ * DM * 2;
constexpr size_t WS_V = WS_K + (size_t)SEQ * DM * 2;
constexpr size_t WS_O = WS_V + (size_t)SEQ * DM * 2;
constexpr size_t WS_ACT = WS_O + (size_t)SEQ * DM * 2;
constexpr size_t WS_KST = WS_ACT + (size_t)SEQ * FF * 2;
constexpr size_t WS_VST = WS_KST + (size_t)NH * NTT * 4;
constexpr size_t WS_QST = WS_VST + (size_t)16 * NTT * 4;
constexpr size_t WS_G = WS_QST + (size_t)NH * NTT * 4;
constexpr size_t WS_FQ = WS_G + (size_t)SEQ * 4;
constexpr size_t WS_FA = WS_FQ + 16 * 256;
constexpr size_t WS_FO = WS_FA + 16 * 256;
constexpr size_t WS_EP = WS_FO + 8 * 256;
constexpr size_t WS_SCR = WS_EP + 256;

DI unsigned cvtpk(float lo, float hi) { f32x2 v = {lo, hi}; return __builtin_bit_cast(unsigned, __builtin_convertvector(v, bf16x2_t)); }
DI float bf2f(u16 v) { return __uint_as_float((unsigned)v << 16); }
DI float bflo(unsigned v) { return __uint_as_float(v << 16); }
DI float bfhi(unsigned v) { return __uint_as_float(v & 0xffff0000u); }
DI void glds16(const void* sbase, unsigned voff, unsigned lds_base) {
    unsigned sv;
    asm volatile("s_mov_b32 %0, m0\n\ts_mov_b32 m0, %3\n\ts_nop 0\n\tglobal_load_lds_dwordx4 %1, %2\n\ts_mov_b32 m0, %0"
                 : "=&s"(sv) : "v"(voff), "s"(sbase), "s"(lds_base) : "memory");
}
DI void glds16c(const void* sbase, unsigned voff, unsigned lds_base) {
    unsigned sv;
    asm volatile("s_mov_b32 %0, m0\n\ts_mov_b32 m0, %3\n\ts_nop 0\n\tglobal_load_lds_dwordx4 %1, %2 sc1\n\ts_mov_b32 m0, %0"
                 : "=&s"(sv) : "v"(voff), "s"(sbase), "s"(lds_base) : "memory");
}
template <int OFF> DI void gload16(bf16x8& dst, const void* sbase, unsigned voff) {
    asm volatile("global_load_dwordx4 %0, %1, %2 offset:%3" : "=v"(dst) : "v"(voff), "s"(sbase), "i"(OFF) : "memory");
}
DI s16x4 trread(lds_cptr p) { return __builtin_bit_cast(s16x4, __builtin_amdgcn_ds_read_tr16_b64_v4i16((__attribute__((address_space(3))) v4i16_t*)p)); }
DI bf16x8 ldsfrag(lds_cptr p) { return *(const __attribute__((address_space(3))) bf16x8*)p; }
DI bf16x8 cat8(s16x4 a, s16x4 b) { return __builtin_shufflevector(a, b, 0, 1, 2, 3, 4, 5, 6, 7); }
template <int N> DI void wait_vm_bar() { asm volatile("s_waitcnt vmcnt(%0)\n\ts_barrier" :: "i"(N) : "memory"); }
template <int N> DI void wait_vm_lgkm_bar() { asm volatile("s_waitcnt vmcnt(%0) lgkmcnt(0)\n\ts_barrier" :: "i"(N) : "memory"); }
DI float xor32f(float v) {
    auto r = __builtin_amdgcn_permlane32_swap(__float_as_uint(v), __float_as_uint(v), false, false);
    return (threadIdx.x & 32) ? __uint_as_float(r[0]) : __uint_as_float(r[1]);
}
DI float dpp_max63(float v) {
#define DSTEP(ctrl, rmask) v = fmaxf(v, __int_as_float(__builtin_amdgcn_update_dpp(__float_as_int(v), __float_as_int(v), ctrl, rmask, 0xf, false)))
    DSTEP(0x111, 0xf); DSTEP(0x112, 0xf); DSTEP(0x114, 0xf); DSTEP(0x118, 0xf); DSTEP(0x142, 0xa); DSTEP(0x143, 0xc);
#undef DSTEP
    return v;
}
DI float dpp_sum63(float v) {
#define DSTEP(ctrl, rmask) v += __int_as_float(__builtin_amdgcn_update_dpp(0, __float_as_int(v), ctrl, rmask, 0xf, false))
    DSTEP(0x111, 0xf); DSTEP(0x112, 0xf); DSTEP(0x114, 0xf); DSTEP(0x118, 0xf); DSTEP(0x142, 0xa); DSTEP(0x143, 0xc);
#undef DSTEP
    return v;
}
DI float bcast63(float v) { return __int_as_float(__builtin_amdgcn_readlane(__float_as_int(v), 63)); }
DI float mish_f(float h) {
    float t = __builtin_amdgcn_exp2f(-fabsf(h) * 1.4426950408889634f);
    float t2 = t * t;
    float num = h > 0.f ? 1.f + 2.f * t : t2 + 2.f * t;
    float den = h > 0.f ? num + 2.f * t2 : num + 2.f;
    return h * num * __builtin_amdgcn_rcpf(den);
}

typedef __attribute__((address_space(1))) unsigned gu32;
#define RLX_AGENT __ATOMIC_RELAXED, __HIP_MEMORY_SCOPE_AGENT
DI void st16_sc1(void* sbase, unsigned voff, u32x4 v) {
    asm volatile("global_store_dwordx4 %0, %1, %2 sc1\n\ts_nop 1" :: "v"(voff), "v"(v), "s"(sbase) : "memory");
}
DI void st4_sc1(void* p, float v) { __hip_atomic_store((gu32*)p, __float_as_uint(v), RLX_AGENT); }
DI void publish(void* flag, unsigned tag) {
    asm volatile("s_waitcnt vmcnt(0) lgkmcnt(0)\n\ts_barrier" ::: "memory");
    if (threadIdx.x == 0) __hip_atomic_store((gu32*)flag, tag, RLX_AGENT);
}
DI void wait_flags(const void* flags, int n, unsigned tag, int lane) {
    for (unsigned spins = 0; spins < (1u << 18); ++spins) {
        unsigned v = tag;
        if (lane < n) v = __hip_atomic_load((const gu32*)flags + lane, RLX_AGENT);
        if (__all(v == tag)) break;
        __builtin_amdgcn_s_sleep(2);
    }
    __builtin_amdgcn_fence(__ATOMIC_ACQUIRE, "wavefront");
    asm volatile("" ::: "memory");
}

constexpr int P_BN = 96, P_ROW = P_BN * 2;
constexpr int G_XS = 16384, G_STG = G_XS + 64 * P_ROW, G_NSL = 5, LDS_BYTES = 163840;
constexpr int A_RG = 32768, A_LC = 4 * A_RG, A_CHAIN = A_LC + 2048;
constexpr int A_LQ = A_CHAIN + 20480, L_CTL = A_LQ + 8192;
constexpr int C_FAST = L_CTL, C_OACQ = L_CTL + 4, C_OCNT = L_CTL + 8, C_WOR = L_CTL + 12, C_CHAIN = L_CTL + 16, C_FULL = L_CTL + 32, C_FREE = L_CTL + 48, C_KV = L_CTL + 64, C_G2 = L_CTL + 80, C_QACQ = L_CTL + 84, C_ACNT = L_CTL + 88, C_CST = L_CTL + 92;
constexpr int M_BASE = 0, M_STG = 16384, M_NS1 = (FF + 63) / 64;
constexpr float A_THR = 40.f;
static_assert(L_CTL >= G_NSL * G_STG && L_CTL + 128 <= LDS_BYTES - 256, "lds map");
typedef __attribute__((address_space(3))) unsigned lu32;
#define RLX_WG __ATOMIC_RELAXED, __HIP_MEMORY_SCOPE_WORKGROUP

DI bf16x8 pack8(const f32x16& p, int s) {
    u32x4 r;
#pragma unroll
    for (int q = 0; q < 4; ++q) r[q] = cvtpk(p[8 * s + 2 * q], p[8 * s + 2 * q + 1]);
    return __builtin_bit_cast(bf16x8, r);
}
#define LGKM_BAR() asm volatile("s_waitcnt lgkmcnt(0)\n\ts_barrier" ::: "memory")
#define BAR() asm volatile("s_barrier" ::: "memory")
template <int N> DI void wait_vm() { asm volatile("s_waitcnt vmcnt(%0)" :: "i"(N) : "memory"); }
DI void raise_flag(void* flag, unsigned tag) { if (threadIdx.x == 0) __hip_atomic_store((gu32*)flag, tag, RLX_AGENT); }
DI void lds_wait_ge(char* lds, int off, unsigned want) {
    asm volatile("" ::: "memory");
    while ((unsigned)__builtin_amdgcn_readfirstlane(__hip_atomic_load((lu32*)((lds_ptr)lds + off), RLX_WG)) < want) __builtin_amdgcn_s_sleep(4);
    asm volatile("" ::: "memory");
}
template <int SLEEP = 1>
DI void lds_wait4_ge(char* lds, int off, unsigned want) {
    asm volatile("" ::: "memory");
    for (;;) {
        const u32x4 v = *(volatile __attribute__((address_space(3))) u32x4*)((lds_ptr)lds + off);
        const unsigned m = min(min(v[0], v[1]), min(v[2], v[3]));
        if ((unsigned)__builtin_amdgcn_readfirstlane(m) >= want) break;
        __builtin_amdgcn_s_sleep(SLEEP);
    }
    asm volatile("" ::: "memory");
}
DI void lds_add1(char* lds, int off, int lane) {
    asm volatile("s_waitcnt lgkmcnt(0)" ::: "memory");
    if (lane == 0) __hip_atomic_fetch_add((lu32*)((lds_ptr)lds + off), 1u, RLX_WG);
}
DI bool try_flags(const void* flags, int n, unsigned tag, int lane) {
    unsigned v = tag;
    if (lane < n) v = __hip_atomic_load((const gu32*)flags + lane, RLX_AGENT);
    if (!__all(v == tag)) return false;
    __builtin_amdgcn_fence(__ATOMIC_ACQUIRE, "agent");
    return true;
}

DI void l2_touch(const void* sbase, unsigned voff, unsigned lds_scratch) {
    unsigned sv;
    asm volatile("s_mov_b32 %0, m0\n\ts_mov_b32 m0, %3\n\ts_nop 0\n\tglobal_load_lds_dword %1, %2\n\ts_mov_b32 m0, %0"
                 : "=&s"(sv) : "v"(voff), "s"(sbase), "s"(lds_scratch) : "memory");
}
DI void l2_touch64(const void* p, unsigned lds_scratch) {
    unsigned sv;
    asm volatile("s_mov_b32 %0, m0\n\ts_mov_b32 m0, %2\n\ts_nop 0\n\tglobal_load_lds_dword %1, off\n\ts_mov_b32 m0, %0"
                 : "=&s"(sv) : "v"(p), "s"(lds_scratch) : "memory");
}
constexpr int L_SCR = 163840 - 256;
struct GFrag { bf16x8 x[4]; bf16x8 w[4]; };
DI void g_rd(GFrag& f, lds_cptr b, const int (&xa)[4], int wa) {
#pragma unroll
    for (int kk = 0; kk < 4; ++kk) {
        f.x[kk] = ldsfrag(b + xa[kk]);
        f.w[kk] = cat8(trread(b + wa + kk * 16 * P_ROW), trread(b + wa + (kk * 16 + 4) * P_ROW));
    }
}
template <bool ISV> DI void g_mma(f32x16& acc, const GFrag& f) {
    SBAR();
#pragma unroll
    for (int kk = 0; kk < 4; ++kk) { if (ISV) acc = MFMA(f.x[kk], f.w[kk], acc); else acc = MFMA(f.w[kk], f.x[kk], acc); }
    PIN(acc);
    SBAR();
}
struct MFrag4 { bf16x8 a[4], w[4]; };
DI void m_rd4(MFrag4& f, lds_cptr A, lds_cptr W) {
#pragma unroll
    for (int kk = 0; kk < 4; ++kk) { f.w[kk] = cat8(trread(W + kk * 2048), trread(W + kk * 2048 + 1024)); f.a[kk] = ldsfrag(A + kk * 1024); }
}

__global__ __launch_bounds__(1024, 4) void fused_kernel(const u16* __restrict__ x, const u16* __restrict__ wqkv, const u16* __restrict__ wo,
                                                        const u16* __restrict__ w1, const u16* __restrict__ b1, const u16* __restrict__ w2,
                                                        const u16* __restrict__ gate_w, char* __restrict__ ws, u16* __restrict__ out) {
    extern __shared__ __attribute__((aligned(16))) char lds[];
    const int tid = threadIdx.x, lane = tid & 63, wid = __builtin_amdgcn_readfirstlane(tid >> 6);
    const int r = lane & 31, hi = lane >> 5;
    const int xg = blockIdx.x & 7, idx = blockIdx.x >> 3;
    const unsigned lds0 = (unsigned)(uintptr_t)lds;
    const unsigned l16 = lane * 16;
    const int i16 = lane & 15, g4 = lane >> 4;
    TS(0);
    const int mt = (xg >> 1) * 4 + (idx & 3), nt = (xg & 1) * 8 + (idx >> 2);
    const int head = xg * 4 + (idx & 3), qg = idx >> 2;
    const int mt4 = xg * 4 + (idx & 3), nt4 = idx >> 2;
    const int m0 = mt * 128;
    unsigned tag;

    if (wid >= 12) {
        const int lw = wid - 12;
        __builtin_amdgcn_s_setprio(3);
        const int rot = ((idx >> 2) + 2 * (idx & 3)) & 7;
        unsigned epv;
        asm volatile("global_load_dword %0, %1, %2 sc1" : "=v"(epv) : "v"(0u), "s"(ws + WS_EP) : "memory");
        unsigned woq[3], wof[3];
#pragma unroll
        for (int j = 0; j < 3; ++j) {
            const int o = (lw * 3 + j) * 1024 + lane * 16;
            const int k = o / P_ROW, cc = (o % P_ROW) >> 4;
            woq[j] = (unsigned)(k * NQKV + (cc >> 2) * 512 + nt * 32 + (cc & 3) * 8) * 2u;
            int n = nt * P_BN + cc * 8;
            n = n > FF - 8 ? FF - 8 : n;
            wof[j] = (unsigned)(k * FF + n) * 2u;
        }
        const unsigned xoff = (unsigned)((m0 + lw * 32 + (lane >> 3)) * DM) * 2u + (((lane & 7) ^ g4) << 4), xoff1 = xoff ^ 64u;
        auto issue = [&](int s, int slot) {
            const int kb = (s + rot) & 7;
            const unsigned base = lds0 + slot * G_STG;
            const u16* xs = x + kb * 64;
            glds16(xs, xoff, base + lw * 4096);
            glds16(xs + 8 * DM, xoff1, base + lw * 4096 + 1024);
            glds16(xs + 16 * DM, xoff, base + lw * 4096 + 2048);
            glds16(xs + 24 * DM, xoff1, base + lw * 4096 + 3072);
            const u16* wsrc = s < 8 ? wqkv + (size_t)kb * 64 * NQKV : w1 + (size_t)kb * 64 * FF;
#pragma unroll
            for (int j = 0; j < 3; ++j) glds16(wsrc, s < 8 ? woq[j] : wof[j], base + G_XS + (lw * 3 + j) * 1024);
        };
        issue(0, 0); issue(1, 1);
        asm volatile("s_waitcnt vmcnt(7)\n\ts_barrier" : "+v"(epv) :: "memory");
        tag = (unsigned)__builtin_amdgcn_readfirstlane(epv) * 0x9E3779B1u + 0x7F4A7C15u;
        issue(2, 2); issue(3, 3);
        wait_vm<14>(); BAR();
        issue(4, 4); issue(5, 0);
        {
            const int ln = idx * 192 + (lw < 3 ? lw * 64 + lane : 0), row = ln / 12;
            int col = (xg & 1) * 768 + (ln - row * 12) * 64;
            col = col > FF - 2 ? FF - 2 : col;
            l2_touch(w1, (unsigned)(row * FF + col) * 2u, lds0 + L_SCR);
        }
        wait_vm<22>(); BAR(); issue(6, 1);
        wait_vm<22>(); BAR(); issue(7, 2);
        wait_vm<22>(); BAR(); issue(8, 3);
        wait_vm<22>(); BAR(); issue(9, 4);
        int slot = 0;
#pragma unroll 1
        for (int s = 5; s < 11; ++s) {
            wait_vm<21>(); BAR();
            issue(s + G_NSL, slot);
            slot = slot + 1 == G_NSL ? 0 : slot + 1;
        }
        wait_vm<21>(); BAR();
        wait_vm<14>(); BAR();
        wait_vm<7>(); BAR();
        wait_vm<0>(); BAR();
        if (lw == 3) {
            wait_flags(ws + WS_FQ + (head >> 1) * 256, 16, tag, lane);
            if (lane == 0) *(volatile lu32*)((lds_ptr)lds + C_QACQ) = 1u;
        }
        BAR();
        float kst, vst, qst;
        asm volatile("global_load_dword %0, %1, %2 sc1" : "=v"(kst) : "v"((unsigned)lane * 4u), "s"(ws + WS_KST + head * 256) : "memory");
        asm volatile("global_load_dword %0, %1, %2 sc1" : "=v"(vst) : "v"((unsigned)lane * 4u), "s"(ws + WS_VST + (head >> 1) * 256) : "memory");
        asm volatile("global_load_dword %0, %1, %2 sc1" : "=v"(qst) : "v"((unsigned)(lane & 7) * 4u), "s"(ws + WS_QST + head * 256 + qg * 32) : "memory");
        glds16c(ws + WS_Q + (size_t)(head * NTT + qg * 8 + lw * 2) * 1024, l16, lds0 + A_LQ + lw * 2048);
        glds16c(ws + WS_Q + (size_t)(head * NTT + qg * 8 + lw * 2 + 1) * 1024, l16, lds0 + A_LQ + lw * 2048 + 1024);
        const char* ksrc = ws + WS_K + (size_t)(head * NTT + lw) * 1024;
        const char* vsrc = ws + WS_V + (size_t)(head * NTT + lw) * 1024;
        auto kv = [&](int t) {
            glds16c(ksrc + t * 1024, l16, lds0 + (t + lw) * 2048);
            glds16c(vsrc + t * 1024, l16, lds0 + (t + lw) * 2048 + 1024);
        };
        auto kv_pub = [&](unsigned n) { if (lane == 0) *(volatile lu32*)((lds_ptr)lds + C_KV + lw * 4) = n; };
        kv(0); kv(4); kv(8); kv(12);
        asm volatile("s_waitcnt vmcnt(10)" : "+v"(kst), "+v"(vst), "+v"(qst) :: "memory");
        if (lw == 0) {
            const float kmax = dpp_max63(kst), vmax = dpp_max63(vst), qmax = dpp_max63(qst);
            if (lane == 63) *(volatile lu32*)((lds_ptr)lds + C_FAST) = (qmax * kmax * 1.01f <= A_THR * A_THR && vmax <= 1e18f) ? 1u : 0u;
            asm volatile("s_waitcnt lgkmcnt(0)" ::: "memory");
        }
        wait_vm<6>(); kv_pub(1);
        kv(16); kv(20); kv(24); kv(28);
        wait_vm<8>(); kv_pub(2);
        kv(32); kv(36); kv(40); kv(44);
        wait_vm<8>(); kv_pub(3);
        kv(48); kv(52); kv(56); kv(60);
#pragma unroll
        for (int j = 0; j < 2; ++j) {
            const int o = (lw * 2 + j) * 64 + lane;
            const int ln = idx * 472 + (o < 472 ? o : 0);
            l2_touch64(ln < 11008 ? (const char*)w2 + (size_t)ln * 128 : (const char*)wo + (size_t)(ln - 11008) * 128, lds0 + L_SCR);
        }
        wait_vm<10>(); kv_pub(4);
        wait_vm<2>(); kv_pub(5);
        __builtin_amdgcn_s_setprio(0);
    } else {
        const int tw = wid & 3, ns = wid >> 2;
        if (tid < 32) *(lu32*)((lds_ptr)lds + L_CTL + tid * 4) = 0u;
        tag = (unsigned)__builtin_amdgcn_readfirstlane(__hip_atomic_load((const gu32*)(ws + WS_EP), RLX_AGENT)) * 0x9E3779B1u + 0x7F4A7C15u;
        const int gtok = m0 + nt * 8 + (wid & 7);
        const u32x4 gx = *(const u32x4*)(x + (size_t)gtok * DM + lane * 8);
        const u32x4 gw = *(const u32x4*)(gate_w + lane * 8);
        float gs = 0.f;
#pragma unroll
        for (int q = 0; q < 4; ++q) { gs = fmaf(bflo(gx[q]), bflo(gw[q]), gs); gs = fmaf(bfhi(gx[q]), bfhi(gw[q]), gs); }
        gs = dpp_sum63(gs);
        PIN(gs);
        const int xrow = tw * 32 + r, xf7 = (xrow >> 1) & 7;
        int xa[4];
#pragma unroll
        for (int kk = 0; kk < 4; ++kk) xa[kk] = xrow * 128 + (((2 * kk + hi) ^ xf7) << 4);
        const int wa = G_XS + ((g4 >> 1) * 8 + (i16 >> 2)) * P_ROW + (ns * 32 + (g4 & 1) * 16 + (i16 & 3) * 4) * 2;
        f32x16 acc = f32x16{};
        GFrag fa, fb;
        LGKM_BAR();
        g_rd(fa, (lds_cptr)lds, xa, wa);
        TS(1);
        int so = G_STG;
#define GSTEP(FC, FN, ISV) { LGKM_BAR(); g_rd(FN, (lds_cptr)lds + so, xa, wa); so = so + G_STG == G_NSL * G_STG ? 0 : so + G_STG; g_mma<ISV>(acc, FC); }
        if (ns == 2) {
#pragma unroll 1
            for (int s = 0; s < 8; s += 2) { GSTEP(fa, fb, true) GSTEP(fb, fa, true) }
        } else {
#pragma unroll 1
            for (int s = 0; s < 8; s += 2) { GSTEP(fa, fb, false) GSTEP(fb, fa, false) }
        }
        TS(2);
        const int tt = mt * 4 + tw, h0 = nt * 2;
        if (ns < 2) {
            const float sc = ns == 0 ? QSCALE : 1.f;
            float ss[2];
#pragma unroll
            for (int e = 0; e < 2; ++e) {
                u32x4 p; float s2 = 0.f;
#pragma unroll
                for (int q = 0; q < 4; ++q) {
                    p[q] = cvtpk(acc[8 * e + 2 * q] * sc, acc[8 * e + 2 * q + 1] * sc);
                    const float a = bflo(p[q]), b = bfhi(p[q]);
                    s2 = fmaf(a, a, s2); s2 = fmaf(b, b, s2);
                }
                st16_sc1(ws + (ns == 0 ? WS_Q : WS_K) + (size_t)((h0 + e) * NTT + tt) * 1024, l16, p);
                ss[e] = s2;
            }
#pragma unroll
            for (int e = 0; e < 2; ++e) {
                ss[e] += xor32f(ss[e]);
                ss[e] = (ss[e] == ss[e]) ? ss[e] : __builtin_inff();
                ss[e] = dpp_max63(ss[e]);
            }
            if (lane == 63) {
                st4_sc1(ws + (ns == 0 ? WS_QST : WS_KST) + (size_t)(h0 * NTT + tt) * 4, ss[0]);
                st4_sc1(ws + (ns == 0 ? WS_QST : WS_KST) + (size_t)((h0 + 1) * NTT + tt) * 4, ss[1]);
            }
        } else {
            const unsigned vo = (unsigned)((r >> 4) * NTT * 1024 + hi * 256 + (r & 15) * 16);
            float vm = 0.f;
#pragma unroll
            for (int s2 = 0; s2 < 2; ++s2) {
                u32x4 pv;
#pragma unroll
                for (int q = 0; q < 4; ++q) {
                    const float a = acc[8 * s2 + 2 * q], b = acc[8 * s2 + 2 * q + 1];
                    pv[q] = cvtpk(a, b);
                    vm = fmaxf(vm, fmaxf((a == a) ? fabsf(a) : __builtin_inff(), (b == b) ? fabsf(b) : __builtin_inff()));
                }
                st16_sc1(ws + WS_V + (size_t)(h0 * NTT + tt) * 1024 + s2 * 512, vo, pv);
            }
            vm = dpp_max63(vm);
            if (lane == 63) st4_sc1(ws + WS_VST + (size_t)(nt * NTT + tt) * 4, vm);
        }
        if (wid < 8 && lane == 63) st4_sc1(ws + WS_G + (size_t)gtok * 4, 1.f / (1.f + __expf(-gs)));
        TS(3);
        acc = f32x16{};
        GSTEP(fa, fb, false)
        wait_vm<0>();
        GSTEP(fb, fa, false)
        raise_flag(ws + WS_FQ + nt * 256 + mt * 4, tag);
        GSTEP(fa, fb, false) GSTEP(fb, fa, false) GSTEP(fa, fb, false) GSTEP(fb, fa, false)
        u32x2 bia[2], bib[2];
        {
            int hi2 = tid; asm volatile("" : "+v"(hi2)); hi2 = (hi2 >> 5) & 1;
#pragma unroll
            for (int e = 0; e < 2; ++e) {
                int f = nt * P_BN + ns * 32 + 16 * e + 4 * hi2;
                f = f < FF ? f : 0;
                bia[e] = *(const u32x2*)(b1 + f);
                bib[e] = *(const u32x2*)(b1 + f + 8);
            }
        }
        GSTEP(fa, fb, false)
#undef GSTEP
        LGKM_BAR();
        if (wid == 0) {
#pragma unroll
            for (int j = 0; j < 2; ++j) {
                const unsigned one2 = ((lane & 15) == 0 || (lane & 15) == 4) ? 0x3f803f80u : 0u;
                *(__attribute__((address_space(3))) u32x4*)((lds_ptr)lds + A_LC + j * 1024 + lane * 16) = u32x4{one2, one2, one2, one2};
            }
            asm volatile("s_waitcnt lgkmcnt(0)" ::: "memory");
            if (lane == 0) *(volatile lu32*)((lds_ptr)lds + C_CST) = 1u;
        }
        g_mma<false>(acc, fb);
        TS(4);
#pragma unroll
        for (int e = 0; e < 2; ++e) {
            const int f0 = nt * P_BN + ns * 32 + 16 * e;
            const u32x2 ba = bia[e], bb = bib[e];
            u32x4 p;
            p[0] = cvtpk(mish_f(acc[8 * e + 0] + bflo(ba[0])), mish_f(acc[8 * e + 1] + bfhi(ba[0])));
            p[1] = cvtpk(mish_f(acc[8 * e + 2] + bflo(ba[1])), mish_f(acc[8 * e + 3] + bfhi(ba[1])));
            p[2] = cvtpk(mish_f(acc[8 * e + 4] + bflo(bb[0])), mish_f(acc[8 * e + 5] + bfhi(bb[0])));
            p[3] = cvtpk(mish_f(acc[8 * e + 6] + bflo(bb[1])), mish_f(acc[8 * e + 7] + bfhi(bb[1])));
            if (f0 < FF) st16_sc1(ws + WS_ACT + (size_t)(tt * FKS + (f0 >> 4)) * 1024, l16, p);
        }
        wait_vm<0>();
        if (lane == 0 && __hip_atomic_fetch_add((lu32*)((lds_ptr)lds + C_ACNT), 1u, RLX_WG) == 11u && nt < 15)
            __hip_atomic_store((gu32*)(ws + WS_FA + mt * 256 + nt * 4), tag, RLX_AGENT);
    }
    const int qp = wid & 3, kq = wid >> 2;
    {
        lds_wait_ge(lds, C_CST, 1u);
        lds_wait4_ge<2>(lds, C_KV, kq == 0 ? 1u : (unsigned)(kq + 2));
        TS(5);
        const bf16x8 qa = ldsfrag((lds_cptr)lds + A_LQ + qp * 2048 + lane * 16), qb = ldsfrag((lds_cptr)lds + A_LQ + qp * 2048 + 1024 + lane * 16);
        const bool fast = __builtin_amdgcn_readfirstlane(*(const lu32*)((lds_cptr)lds + C_FAST)) != 0;
        f32x16 oa = f32x16{}, ob = f32x16{};
        float ma = 0.f, mb = 0.f;
        const int kbase = kq * A_RG + lane * 16;
        const int vbase = r < 16 ? kq * A_RG + 1024 + hi * 256 + r * 16 : A_LC + hi * 256 + (r - 16) * 16;
        const int vinc = r < 16 ? 4096 : 0;
        const int voff1 = r < 16 ? 2048 : 1024;
        const f32x16 zero = f32x16{};
        if (fast) {
            lds_cptr kc = (lds_cptr)lds + kbase, vc = (lds_cptr)lds + vbase, vd = vc + voff1;
            bf16x8 kf = ldsfrag(kc);
            bf16x8 va0 = ldsfrag(vc), va1 = ldsfrag(vc + 512), vb0, vb1;
            f32x16 sa = MFMA(kf, qa, zero), sb = MFMA(kf, qb, zero);
            kf = ldsfrag(kc + 2048);
#define HALF(S, O, V0, V1)                                                              \
                _Pragma("unroll")                                                       \
                for (int i = 0; i < 8; ++i) S[i] = __builtin_amdgcn_exp2f(S[i]);        \
                O = MFMA(V0, pack8(S, 0), O);                                           \
                PIN(O); SBAR();                                                         \
                _Pragma("unroll")                                                       \
                for (int i = 8; i < 16; ++i) S[i] = __builtin_amdgcn_exp2f(S[i]);       \
                O = MFMA(V1, pack8(S, 1), O);                                           \
                PIN(O); SBAR();
#define TILE(VC0, VC1, VN0, VN1, KNEXT, VNEXT)                                          \
            {                                                                           \
                VN0 = ldsfrag(VNEXT); VN1 = ldsfrag((VNEXT) + 512);                     \
                SBAR();                                                                 \
                HALF(sa, oa, VC0, VC1)                                                  \
                sa = MFMA(kf, qa, zero); PIN(sa); SBAR();                               \
                HALF(sb, ob, VC0, VC1)                                                  \
                sb = MFMA(kf, qb, zero); PIN(sb);                                       \
                kf = ldsfrag(KNEXT);                                                    \
                SBAR();                                                                 \
            }
#pragma unroll 1
            for (int t = 0; t < 16; t += 2) {
                if (t == 2 && kq == 0) lds_wait4_ge(lds, C_KV, 2u);
                const int kn = t < 14 ? 4096 : 0;
                TILE(va0, va1, vb0, vb1, kc + kn, vd)
                vc += t < 14 ? vinc : 0;
                TILE(vb0, vb1, va0, va1, kc + kn + 2048, vc)
                kc += kn; vd += vinc;
            }
#undef TILE
#undef HALF
        } else {
            ma = -__builtin_inff(); mb = ma;
#pragma unroll 1
            for (int t = 0; t < 16; ++t) {
                if (t == 4 && kq == 0) lds_wait4_ge(lds, C_KV, 2u);
                lds_cptr vc = (lds_cptr)lds + vbase + (r < 16 ? t * 2048 : 0);
                const bf16x8 kf = ldsfrag((lds_cptr)lds + kbase + t * 2048);
                const bf16x8 v0 = ldsfrag(vc), v1 = ldsfrag(vc + 512);
#define SLOW(Q, O, M)                                                                   \
                {                                                                       \
                    f32x16 s = MFMA(kf, Q, zero);                                       \
                    float mx = s[0];                                                    \
                    _Pragma("unroll")                                                   \
                    for (int i = 1; i < 16; ++i) mx = fmaxf(mx, s[i]);                  \
                    mx = fmaxf(mx, xor32f(mx));                                         \
                    const float mn = fmaxf(M, mx);                                      \
                    const float al = __builtin_amdgcn_exp2f(M - mn);                    \
                    M = mn;                                                             \
                    _Pragma("unroll")                                                   \
                    for (int i = 0; i < 16; ++i) { s[i] = __builtin_amdgcn_exp2f(s[i] - mn); O[i] *= al; } \
                    O = MFMA(v0, pack8(s, 0), O);                                       \
                    O = MFMA(v1, pack8(s, 1), O);                                       \
                }
                SLOW(qa, oa, ma)
                SLOW(qb, ob, mb)
#undef SLOW
            }
        }
        TS(6);
        {
            int tz = tid; asm volatile("" : "+v"(tz));
            lds_ptr ch = (lds_ptr)lds + A_CHAIN + qp * 5120 + (tz & 63) * 16;
            lds_ptr cl = (lds_ptr)lds + A_CHAIN + qp * 5120 + 4096 + (tz & 63) * 16;
            if (kq > 0) {
                lds_wait_ge(lds, C_CHAIN + qp * 4, (unsigned)kq);
                const f32x4 a0 = *(__attribute__((address_space(3))) f32x4*)(ch), a1 = *(__attribute__((address_space(3))) f32x4*)(ch + 1024);
                const f32x4 b0 = *(__attribute__((address_space(3))) f32x4*)(ch + 2048), b1v = *(__attribute__((address_space(3))) f32x4*)(ch + 3072);
                const f32x4 lm = *(__attribute__((address_space(3))) f32x4*)(cl);
                const float na = fmaxf(ma, lm[1]), nb = fmaxf(mb, lm[3]);
                const float fa = __builtin_amdgcn_exp2f(ma - na), xa2 = __builtin_amdgcn_exp2f(lm[1] - na);
                const float fb = __builtin_amdgcn_exp2f(mb - nb), xb2 = __builtin_amdgcn_exp2f(lm[3] - nb);
#pragma unroll
                for (int e = 0; e < 4; ++e) {
                    oa[e] = fmaf(a0[e], xa2, oa[e] * fa); oa[4 + e] = fmaf(a1[e], xa2, oa[4 + e] * fa);
                    ob[e] = fmaf(b0[e], xb2, ob[e] * fb); ob[4 + e] = fmaf(b1v[e], xb2, ob[4 + e] * fb);
                }
                oa[8] = fmaf(lm[0], xa2, oa[8] * fa); ob[8] = fmaf(lm[2], xb2, ob[8] * fb);
                ma = na; mb = nb;
            }
            if (kq < 3) {
                *(__attribute__((address_space(3))) f32x4*)(ch) = f32x4{oa[0], oa[1], oa[2], oa[3]};
                *(__attribute__((address_space(3))) f32x4*)(ch + 1024) = f32x4{oa[4], oa[5], oa[6], oa[7]};
                *(__attribute__((address_space(3))) f32x4*)(ch + 2048) = f32x4{ob[0], ob[1], ob[2], ob[3]};
                *(__attribute__((address_space(3))) f32x4*)(ch + 3072) = f32x4{ob[4], ob[5], ob[6], ob[7]};
                *(__attribute__((address_space(3))) f32x4*)(cl) = f32x4{oa[8], ma, ob[8], mb};
                lds_add1(lds, C_CHAIN + qp * 4, lane);
            } else {
                const float ra = 1.f / oa[8], rb = 1.f / ob[8];
                u32x4 pa, pb;
#pragma unroll
                for (int q = 0; q < 4; ++q) { pa[q] = cvtpk(oa[2 * q] * ra, oa[2 * q + 1] * ra); pb[q] = cvtpk(ob[2 * q] * rb, ob[2 * q + 1] * rb); }
                const int qt = qg * 8 + qp * 2;
                st16_sc1(ws + WS_O + (size_t)(qt * NH + head) * 1024, l16, pa);
                st16_sc1(ws + WS_O + (size_t)((qt + 1) * NH + head) * 1024, l16, pb);
                wait_vm<0>();
            }
            if (kq >= 2) {
                unsigned prev = 0;
                lu32* cnt = (lu32*)((lds_ptr)lds + C_OCNT) + (kq == 2 ? (C_G2 - C_OCNT) / 4 : 0);
                if (lane == 0) prev = __hip_atomic_fetch_add(cnt, 1u, RLX_WG);
                if (__builtin_amdgcn_readfirstlane(prev) == 3) {
                    if (kq == 3 && lane == 0) __hip_atomic_store((gu32*)(ws + WS_FO + qg * 256 + head * 4), tag, RLX_AGENT);
                    const unsigned wo_l = (unsigned)(((kq - 2) * 256 + ((tz >> 5) & 1) * 4 + ((tz >> 2) & 3)) * DM + nt4 * 64 + ((tz >> 4) & 1) * 32 + (tz & 3) * 8) * 2u;
#pragma unroll 1
                    for (int pc = 0; pc < 32; pc += 4) {
#pragma unroll
                        for (int j = 0; j < 4; ++j) glds16(wo + (size_t)(pc + j) * 8 * DM, wo_l, lds0 + kq * A_RG + (pc + j) * 1024);
                    }
                    wait_vm<0>();
                    lds_add1(lds, C_WOR, lane);
                }
            }
        }
        TS(7);
    }
    if (kq >= 2) return;
    {
        int tz = tid; asm volatile("" : "+v"(tz));
        const unsigned lz16 = (tz & 63) * 16;
        if (kq == 0) {
            const char* aF = ws + WS_ACT + (size_t)(mt4 * 2 * FKS + qp) * 1024;
            const unsigned wo_l = (unsigned)((qp * 8 + ((tz >> 5) & 1) * 4 + ((tz >> 2) & 3)) * DM + nt4 * 64 + ((tz >> 4) & 1) * 32 + (tz & 3) * 8) * 2u;
            const unsigned lb = lds0 + M_BASE + qp * 1024;
            auto issue = [&](const char* a, const u16* w, int wadj, int s) {
                const unsigned base = lb + (s & 3) * M_STG;
                glds16c(a, lz16, base);
                glds16c(a + (size_t)FKS * 1024, lz16, base + 4096);
                glds16(w, wo_l, base + 8192);
                glds16(w + wadj * DM, wo_l, base + 12288);
            };
            auto publish_full = [&](int n) { if (lane == 0) *(volatile lu32*)((lds_ptr)lds + C_FULL + qp * 4) = (unsigned)n; };
            if (qp == 0) {
                wait_flags(ws + WS_FA + (mt4 >> 1) * 256, 15, tag, lane);
                if (lane == 0) *(volatile lu32*)((lds_ptr)lds + C_OACQ) = 1u;
#pragma unroll
                for (int j = 0; j < 3; ++j) {
                    const int o = j * 64 + lane;
                    l2_touch(ws + WS_ACT + (size_t)mt4 * 2 * FKS * 1024, (unsigned)(nt4 * 172 + (o < 172 ? o : 0)) * 128u, lds0 + L_SCR);
                }
            } else {
                lds_wait_ge(lds, C_OACQ, 1u);
            }
            lds_wait4_ge<4>(lds, C_CHAIN, 1u);
            issue(aF, w2, 32, 0); issue(aF + 4096, w2 + (size_t)64 * DM, 32, 1);
            wait_vm<0>(); publish_full(2);
            lds_wait4_ge<8>(lds, C_CHAIN, 2u);
            issue(aF + 2 * 4096, w2 + (size_t)2 * 64 * DM, 32, 2); issue(aF + 3 * 4096, w2 + (size_t)3 * 64 * DM, 32, 3);
            TS(8);
#pragma unroll 1
            for (int s = 4; s < M_NS1; ++s) {
                wait_vm<8>(); publish_full(s - 2);
                lds_wait4_ge(lds, C_FREE, (unsigned)(s - 3));
                const bool lastst = s == M_NS1 - 1;
                issue(aF + (size_t)s * 4096 - ((lastst && (qp & 2)) ? 2048 : 0), w2 + (size_t)s * 64 * DM, lastst ? 0 : 32, s);
            }
            wait_vm<8>(); publish_full(M_NS1 - 2);
            wait_vm<4>(); publish_full(M_NS1 - 1);
            wait_vm<0>(); publish_full(M_NS1);
            TS(11);
            if (qp == 0) {
                wait_flags(ws + WS_FO + (mt4 >> 2) * 256, 32, tag, lane);
                if (lane == 0) *(volatile lu32*)((lds_ptr)lds + C_OACQ) = 2u;
            } else {
                lds_wait_ge(lds, C_OACQ, 2u);
            }
            const char* aO = ws + WS_O + (size_t)(mt4 * 2 * NH + qp) * 1024;
#pragma unroll
            for (int j = 0; j < 4; ++j) {
                const int s = M_NS1 + j;
                lds_wait4_ge(lds, C_FREE, (unsigned)(s - 3));
                const unsigned base = lb + (s & 3) * M_STG;
                glds16c(aO + (size_t)j * 8192, lz16, base);
                glds16c(aO + (size_t)j * 8192 + 4096, lz16, base + 4096);
                glds16c(aO + (size_t)(NH + j * 8) * 1024, lz16, base + 8192);
                glds16c(aO + (size_t)(NH + j * 8) * 1024 + 4096, lz16, base + 12288);
            }
            wait_vm<12>(); publish_full(M_NS1 + 1);
            wait_vm<8>(); publish_full(M_NS1 + 2);
            wait_vm<4>(); publish_full(M_NS1 + 3);
            wait_vm<0>(); publish_full(M_NS1 + 4);
        } else {
            const int wm = qp & 1, wn = qp >> 1;
            const int wl = (((tz >> 5) & 1) * 2 + wn) * 256 + ((tz >> 2) & 3) * 64 + ((tz >> 4) & 1) * 32 + (tz & 3) * 8;
            lds_cptr W = (lds_cptr)lds + M_BASE + 8192 + wl;
            lds_cptr A = (lds_cptr)lds + M_BASE + wm * 4096 + lz16;
            f32x16 acca = f32x16{}, accb = f32x16{};
            MFrag4 fa, fb;
            auto release = [&](int n) {
                asm volatile("s_waitcnt lgkmcnt(0)" ::: "memory");
                if (lane == 0) *(volatile lu32*)((lds_ptr)lds + C_FREE + qp * 4) = (unsigned)n;
            };
            lds_wait4_ge(lds, C_FULL, 1u);
            m_rd4(fa, A, W);
            release(1);
#define MIT(S, FC, FN, NK)                                                                                               \
            {                                                                                                            \
                if ((S) + 1 < M_NS1) {                                                                                   \
                    lds_wait4_ge(lds, C_FULL, (unsigned)((S) + 2));                                                      \
                    m_rd4(FN, A + (((S) + 1) & 3) * M_STG, W + (((S) + 1) & 3) * M_STG);                                 \
                }                                                                                                        \
                SBAR();                                                                                                  \
                _Pragma("unroll")                                                                                        \
                for (int kk = 0; kk < NK; ++kk) accb = MFMA(FC.w[kk], FC.a[kk], accb);                                   \
                PIN(accb);                                                                                               \
                SBAR();                                                                                                  \
                if ((S) + 1 < M_NS1) release((S) + 2);                                                                   \
            }
#pragma unroll 1
            for (int s = 0; s < M_NS1 - 2; s += 2) { MIT(s, fa, fb, 4) MIT(s + 1, fb, fa, 4) }
            MIT(M_NS1 - 2, fa, fb, 4) MIT(M_NS1 - 1, fb, fa, 2)
#undef MIT
            lds_wait_ge(lds, C_WOR, 2u);
            float g = 0.f;
            lds_cptr AO = (lds_cptr)lds + M_BASE + wm * 8192 + lz16;
#pragma unroll
            for (int j = 0; j < 4; ++j) {
                lds_wait4_ge(lds, C_FULL, (unsigned)(M_NS1 + j + 1));
                if (j == 0) asm volatile("global_load_dword %0, %1, %2 sc1" : "=v"(g) : "v"((unsigned)(mt4 * 64 + wm * 32 + (tz & 31)) * 4u), "s"(ws + WS_G) : "memory");
                lds_cptr Wj = (lds_cptr)lds + 2 * A_RG + j * 16384 + wl;
                m_rd4(fa, AO + ((M_NS1 + j) & 3) * M_STG, Wj);
                m_rd4(fb, AO + ((M_NS1 + j) & 3) * M_STG + 4096, Wj + 8192);
#pragma unroll
                for (int kk = 0; kk < 4; ++kk) acca = MFMA(fa.w[kk], fa.a[kk], acca);
#pragma unroll
                for (int kk = 0; kk < 4; ++kk) acca = MFMA(fb.w[kk], fb.a[kk], acca);
            }
            asm volatile("s_waitcnt vmcnt(0)" : "+v"(g) :: "memory");
            TS(9);
            const float g1 = 1.f - g;
            u16* orow = out + (size_t)(mt4 * 64 + wm * 32 + (tz & 31)) * DM + nt4 * 64 + wn * 32 + ((tz >> 5) & 1) * 4;
#pragma unroll
            for (int q = 0; q < 4; ++q) {
                u32x2 p;
                p[0] = cvtpk(g * acca[4 * q] + g1 * accb[4 * q], g * acca[4 * q + 1] + g1 * accb[4 * q + 1]);
                p[1] = cvtpk(g * acca[4 * q + 2] + g1 * accb[4 * q + 2], g * acca[4 * q + 3] + g1 * accb[4 * q + 3]);
                *(u32x2*)(orow + q * 8) = p;
            }
        }
    }
    if (blockIdx.x == 0 && tid == 0) __hip_atomic_store((gu32*)(ws + WS_EP), tag, RLX_AGENT);
    TS(10);
}

extern "C" void kernel_launch(void* const* d_in, const int* in_sizes, int n_in, void* d_out, int out_size, void* d_ws, size_t ws_size,
                              hipStream_t stream) {
    (void)hipFuncSetAttribute((const void*)fused_kernel, hipFuncAttributeMaxDynamicSharedMemorySize, LDS_BYTES);
    fused_kernel<<<256, 1024, LDS_BYTES, stream>>>((const u16*)d_in[0], (const u16*)d_in[1], (const u16*)d_in[2], (const u16*)d_in[4],
                                                   (const u16*)d_in[5], (const u16*)d_in[6], (const u16*)d_in[7], (char*)d_ws, (u16*)d_out);
}
```

```cpp
#include <hip/hip_runtime.h>
#include <hip/hip_bf16.h>
#include <stdint.h>

using bf16x8 = __attribute__((ext_vector_type(8))) short;
using s16x4  = __attribute__((ext_vector_type(4))) short;
using f32x16 = __attribute__((ext_vector_type(16))) float;
using f32x4  = __attribute__((ext_vector_type(4))) float;
using f32x2  = __attribute__((ext_vector_type(2))) float;
using u32x4  = __attribute__((ext_vector_type(4))) unsigned;
using u32x2  = __attribute__((ext_vector_type(2))) unsigned;
typedef __bf16 bf16x2_t __attribute__((ext_vector_type(2)));
typedef short v4i16_t __attribute__((ext_vector_type(4)));
typedef __attribute__((address_space(3))) const char* lds_cptr;
typedef __attribute__((address_space(3))) char* lds_ptr;
typedef unsigned short u16;

#define DI __device__ __forceinline__
#define TS(i)
#define TSEND(k)
#define MFMA(a, b, c) __builtin_amdgcn_mfma_f32_32x32x16_bf16((a), (b), (c), 0, 0, 0)
#define SBAR() __builtin_amdgcn_sched_barrier(0)
#define PIN(x) asm volatile("" : "+v"(x))

constexpr int SEQ = 2048, DM = 512, NH = 32, DH = 16, FF = 1376, NQKV = 1536;
constexpr int NTT = SEQ / 32;
constexpr int FKS = FF / 16;
constexpr float QSCALE = 0.25f * 1.4426950408889634f;

constexpr size_t WS_Q = 0;
constexpr size_t WS_K = WS_Q + (size_t)SEQ * DM * 2;
constexpr size_t WS_V = WS_K + (size_t)SEQ * DM * 2;
constexpr size_t WS_O = WS_V + (size_t)SEQ * DM * 2;
constexpr size_t WS_ACT = WS_O + (size_t)SEQ * DM * 2;
constexpr size_t WS_KST = WS_ACT + (size_t)SEQ * FF * 2;
constexpr size_t WS_VST = WS_KST + (size_t)NH * NTT * 4;
constexpr size_t WS_QST = WS_VST + (size_t)16 * NTT * 4;
constexpr size_t WS_G = WS_QST + (size_t)NH * NTT * 4;
constexpr size_t WS_FQ = WS_G + (size_t)SEQ * 4;
constexpr size_t WS_FA = WS_FQ + 16 * 256;
constexpr size_t WS_FO = WS_FA + 16 * 256;
constexpr size_t WS_EP = WS_FO + 8 * 256;
constexpr size_t WS_SCR = WS_EP + 256;

DI unsigned cvtpk(float lo, float hi) { f32x2 v = {lo, hi}; return __builtin_bit_cast(unsigned, __builtin_convertvector(v, bf16x2_t)); }
DI float bf2f(u16 v) { return __uint_as_float((unsigned)v << 16); }
DI float bflo(unsigned v) { return __uint_as_float(v << 16); }
DI float bfhi(unsigned v) { return __uint_as_float(v & 0xffff0000u); }
DI void glds16(const void* sbase, unsigned voff, unsigned lds_base) {
    unsigned sv;
    asm volatile("s_mov_b32 %0, m0\n\ts_mov_b32 m0, %3\n\ts_nop 0\n\tglobal_load_lds_dwordx4 %1, %2\n\ts_mov_b32 m0, %0"
                 : "=&s"(sv) : "v"(voff), "s"(sbase), "s"(lds_base) : "memory");
}
DI void glds16c(const void* sbase, unsigned voff, unsigned lds_base) {
    unsigned sv;
    asm volatile("s_mov_b32 %0, m0\n\ts_mov_b32 m0, %3\n\ts_nop 0\n\tglobal_load_lds_dwordx4 %1, %2 sc1\n\ts_mov_b32 m0, %0"
                 : "=&s"(sv) : "v"(voff), "s"(sbase), "s"(lds_base) : "memory");
}
template <int OFF> DI void gload16(bf16x8& dst, const void* sbase, unsigned voff) {
    asm volatile("global_load_dwordx4 %0, %1, %2 offset:%3" : "=v"(dst) : "v"(voff), "s"(sbase), "i"(OFF) : "memory");
}
DI s16x4 trread(lds_cptr p) { return __builtin_bit_cast(s16x4, __builtin_amdgcn_ds_read_tr16_b64_v4i16((__attribute__((address_space(3))) v4i16_t*)p)); }
DI bf16x8 ldsfrag(lds_cptr p) { return *(const __attribute__((address_space(3))) bf16x8*)p; }
DI bf16x8 cat8(s16x4 a, s16x4 b) { return __builtin_shufflevector(a, b, 0, 1, 2, 3, 4, 5, 6, 7); }
template <int N> DI void wait_vm_bar() { asm volatile("s_waitcnt vmcnt(%0)\n\ts_barrier" :: "i"(N) : "memory"); }
template <int N> DI void wait_vm_lgkm_bar() { asm volatile("s_waitcnt vmcnt(%0) lgkmcnt(0)\n\ts_barrier" :: "i"(N) : "memory"); }
DI float xor32f(float v) {
    auto r = __builtin_amdgcn_permlane32_swap(__float_as_uint(v), __float_as_uint(v), false, false);
    return (threadIdx.x & 32) ? __uint_as_float(r[0]) : __uint_as_float(r[1]);
}
DI float dpp_max63(float v) {
#define DSTEP(ctrl, rmask) v = fmaxf(v, __int_as_float(__builtin_amdgcn_update_dpp(__float_as_int(v), __float_as_int(v), ctrl, rmask, 0xf, false)))
    DSTEP(0x111, 0xf); DSTEP(0x112, 0xf); DSTEP(0x114, 0xf); DSTEP(0x118, 0xf); DSTEP(0x142, 0xa); DSTEP(0x143, 0xc);
#undef DSTEP
    return v;
}
DI float dpp_sum63(float v) {
#define DSTEP(ctrl, rmask) v += __int_as_float(__builtin_amdgcn_update_dpp(0, __float_as_int(v), ctrl, rmask, 0xf, false))
    DSTEP(0x111, 0xf); DSTEP(0x112, 0xf); DSTEP(0x114, 0xf); DSTEP(0x118, 0xf); DSTEP(0x142, 0xa); DSTEP(0x143, 0xc);
#undef DSTEP
    return v;
}
DI float bcast63(float v) { return __int_as_float(__builtin_amdgcn_readlane(__float_as_int(v), 63)); }
DI float mish_f(float h) {
    float t = __builtin_amdgcn_exp2f(-fabsf(h) * 1.4426950408889634f);
    float t2 = t * t;
    float num = h > 0.f ? 1.f + 2.f * t : t2 + 2.f * t;
    float den = h > 0.f ? num + 2.f * t2 : num + 2.f;
    return h * num * __builtin_amdgcn_rcpf(den);
}

typedef __attribute__((address_space(1))) unsigned gu32;
#define RLX_AGENT __ATOMIC_RELAXED, __HIP_MEMORY_SCOPE_AGENT
DI void st16_sc1(void* sbase, unsigned voff, u32x4 v) {
    asm volatile("global_store_dwordx4 %0, %1, %2 sc1\n\ts_nop 1" :: "v"(voff), "v"(v), "s"(sbase) : "memory");
}
DI void st4_sc1(void* p, float v) { __hip_atomic_store((gu32*)p, __float_as_uint(v), RLX_AGENT); }
DI void publish(void* flag, unsigned tag) {
    asm volatile("s_waitcnt vmcnt(0) lgkmcnt(0)\n\ts_barrier" ::: "memory");
    if (threadIdx.x == 0) __hip_atomic_store((gu32*)flag, tag, RLX_AGENT);
}
DI void wait_flags(const void* flags, int n, unsigned tag, int lane) {
    for (unsigned spins = 0; spins < (1u << 18); ++spins) {
        unsigned v = tag;
        if (lane < n) v = __hip_atomic_load((const gu32*)flags + lane, RLX_AGENT);
        if (__all(v == tag)) break;
        __builtin_amdgcn_s_sleep(2);
    }
    __builtin_amdgcn_fence(__ATOMIC_ACQUIRE, "wavefront");
    asm volatile("" ::: "memory");
}

constexpr int P_BN = 96, P_ROW = P_BN * 2;
constexpr int G_XS = 16384, G_STG = G_XS + 64 * P_ROW, G_NSL = 5, LDS_BYTES = 163840;
constexpr int A_RG = 32768, A_LC = 4 * A_RG, A_CHAIN = A_LC + 2048;
constexpr int A_LQ = A_CHAIN + 20480, L_CTL = A_LQ + 8192;
constexpr int C_FAST = L_CTL, C_OACQ = L_CTL + 4, C_OCNT = L_CTL + 8, C_WOR = L_CTL + 12, C_CHAIN = L_CTL + 16, C_FULL = L_CTL + 32, C_FREE = L_CTL + 48, C_KV = L_CTL + 64, C_G2 = L_CTL + 80, C_QACQ = L_CTL + 84, C_ACNT = L_CTL + 88, C_CST = L_CTL + 92;
constexpr int M_BASE = 0, M_STG = 16384, M_NS1 = (FF + 63) / 64;
constexpr float A_THR = 40.f;
static_assert(L_CTL >= G_NSL * G_STG && L_CTL + 128 <= LDS_BYTES - 256, "lds map");
typedef __attribute__((address_space(3))) unsigned lu32;
#define RLX_WG __ATOMIC_RELAXED, __HIP_MEMORY_SCOPE_WORKGROUP

DI bf16x8 pack8(const f32x16& p, int s) {
    u32x4 r;
#pragma unroll
    for (int q = 0; q < 4; ++q) r[q] = cvtpk(p[8 * s + 2 * q], p[8 * s + 2 * q + 1]);
    return __builtin_bit_cast(bf16x8, r);
}
#define LGKM_BAR() asm volatile("s_waitcnt lgkmcnt(0)\n\ts_barrier" ::: "memory")
#define BAR() asm volatile("s_barrier" ::: "memory")
template <int N> DI void wait_vm() { asm volatile("s_waitcnt vmcnt(%0)" :: "i"(N) : "memory"); }
DI void raise_flag(void* flag, unsigned tag) { if (threadIdx.x == 0) __hip_atomic_store((gu32*)flag, tag, RLX_AGENT); }
DI void lds_wait_ge(char* lds, int off, unsigned want) {
    asm volatile("" ::: "memory");
    while ((unsigned)__builtin_amdgcn_readfirstlane(__hip_atomic_load((lu32*)((lds_ptr)lds + off), RLX_WG)) < want) __builtin_amdgcn_s_sleep(4);
    asm volatile("" ::: "memory");
}
template <int SLEEP = 1>
DI void lds_wait4_ge(char* lds, int off, unsigned want) {
    asm volatile("" ::: "memory");
    for (;;) {
        const u32x4 v = *(volatile __attribute__((address_space(3))) u32x4*)((lds_ptr)lds + off);
        const unsigned m = min(min(v[0], v[1]), min(v[2], v[3]));
        if ((unsigned)__builtin_amdgcn_readfirstlane(m) >= want) break;
        __builtin_amdgcn_s_sleep(SLEEP);
    }
    asm volatile("" ::: "memory");
}
DI void lds_add1(char* lds, int off, int lane) {
    asm volatile("s_waitcnt lgkmcnt(0)" ::: "memory");
    if (lane == 0) __hip_atomic_fetch_add((lu32*)((lds_ptr)lds + off), 1u, RLX_WG);
}
DI bool try_flags(const void* flags, int n, unsigned tag, int lane) {
    unsigned v = tag;
    if (lane < n) v = __hip_atomic_load((const gu32*)flags + lane, RLX_AGENT);
    if (!__all(v == tag)) return false;
    __builtin_amdgcn_fence(__ATOMIC_ACQUIRE, "agent");
    return true;
}

DI void l2_touch(const void* sbase, unsigned voff, unsigned lds_scratch) {
    unsigned sv;
    asm volatile("s_mov_b32 %0, m0\n\ts_mov_b32 m0, %3\n\ts_nop 0\n\tglobal_load_lds_dword %1, %2\n\ts_mov_b32 m0, %0"
                 : "=&s"(sv) : "v"(voff), "s"(sbase), "s"(lds_scratch) : "memory");
}
DI void l2_touch64(const void* p, unsigned lds_scratch) {
    unsigned sv;
    asm volatile("s_mov_b32 %0, m0\n\ts_mov_b32 m0, %2\n\ts_nop 0\n\tglobal_load_lds_dword %1, off\n\ts_mov_b32 m0, %0"
                 : "=&s"(sv) : "v"(p), "s"(lds_scratch) : "memory");
}
constexpr int L_SCR = 163840 - 256;
struct GFrag { bf16x8 x[4]; bf16x8 w[4]; };
DI void g_rd(GFrag& f, lds_cptr b, const int (&xa)[4], int wa) {
#pragma unroll
    for (int kk = 0; kk < 4; ++kk) {
        f.x[kk] = ldsfrag(b + xa[kk]);
        f.w[kk] = cat8(trread(b + wa + kk * 16 * P_ROW), trread(b + wa + (kk * 16 + 4) * P_ROW));
    }
}
template <bool ISV> DI void g_mma(f32x16& acc, const GFrag& f) {
    SBAR();
#pragma unroll
    for (int kk = 0; kk < 4; ++kk) { if (ISV) acc = MFMA(f.x[kk], f.w[kk], acc); else acc = MFMA(f.w[kk], f.x[kk], acc); }
    PIN(acc);
    SBAR();
}
struct MFrag4 { bf16x8 a[4], w[4]; };
DI void m_rd4(MFrag4& f, lds_cptr A, lds_cptr W) {
#pragma unroll
    for (int kk = 0; kk < 4; ++kk) { f.w[kk] = cat8(trread(W + kk * 2048), trread(W + kk * 2048 + 1024)); f.a[kk] = ldsfrag(A + kk * 1024); }
}

__global__ __launch_bounds__(1024, 4) void fused_kernel(const u16* __restrict__ x, const u16* __restrict__ wqkv, const u16* __restrict__ wo,
                                                        const u16* __restrict__ w1, const u16* __restrict__ b1, const u16* __restrict__ w2,
                                                        const u16* __restrict__ gate_w, char* __restrict__ ws, u16* __restrict__ out) {
    extern __shared__ __attribute__((aligned(16))) char lds[];
    const int tid = threadIdx.x, lane = tid & 63, wid = __builtin_amdgcn_readfirstlane(tid >> 6);
    const int r = lane & 31, hi = lane >> 5;
    const int xg = blockIdx.x & 7, idx = blockIdx.x >> 3;
    const unsigned lds0 = (unsigned)(uintptr_t)lds;
    const unsigned l16 = lane * 16;
    const int i16 = lane & 15, g4 = lane >> 4;
    TS(0);
    const int mt = (xg >> 1) * 4 + (idx & 3), nt = (xg & 1) * 8 + (idx >> 2);
    const int head = xg * 4 + (idx & 3), qg = idx >> 2;
    const int mt4 = xg * 4 + (idx & 3), nt4 = idx >> 2;
    const int m0 = mt * 128;
    unsigned tag;

    if (wid >= 12) {
        const int lw = wid - 12;
        __builtin_amdgcn_s_setprio(3);
        const int rot = ((idx >> 2) + 2 * (idx & 3)) & 7;
        unsigned epv;
        asm volatile("global_load_dword %0, %1, %2 sc1" : "=v"(epv) : "v"(0u), "s"(ws + WS_EP) : "memory");
        unsigned woq[3], wof[3];
#pragma unroll
        for (int j = 0; j < 3; ++j) {
            const int o = (lw * 3 + j) * 1024 + lane * 16;
            const int k = o / P_ROW, cc = (o % P_ROW) >> 4;
            woq[j] = (unsigned)(k * NQKV + (cc >> 2) * 512 + nt * 32 + (cc & 3) * 8) * 2u;
            int n = nt * P_BN + cc * 8;
            n = n > FF - 8 ? FF - 8 : n;
            wof[j] = (unsigned)(k * FF + n) * 2u;
        }
        const unsigned xoff = (unsigned)((m0 + lw * 32 + (lane >> 3)) * DM) * 2u + (((lane & 7) ^ g4) << 4), xoff1 = xoff ^ 64u;
        auto issue = [&](int s, int slot) {
            const int kb = (s + rot) & 7;
            const unsigned base = lds0 + slot * G_STG;
            const u16* xs = x + kb * 64;
            glds16(xs, xoff, base + lw * 4096);
            glds16(xs + 8 * DM, xoff1, base + lw * 4096 + 1024);
            glds16(xs + 16 * DM, xoff, base + lw * 4096 + 2048);
            glds16(xs + 24 * DM, xoff1, base + lw * 4096 + 3072);
            const u16* wsrc = s < 8 ? wqkv + (size_t)kb * 64 * NQKV : w1 + (size_t)kb * 64 * FF;
#pragma unroll
            for (int j = 0; j < 3; ++j) glds16(wsrc, s < 8 ? woq[j] : wof[j], base + G_XS + (lw * 3 + j) * 1024);
        };
        issue(0, 0); issue(1, 1);
        asm volatile("s_waitcnt vmcnt(7)\n\ts_barrier" : "+v"(epv) :: "memory");
        tag = (unsigned)__builtin_amdgcn_readfirstlane(epv) * 0x9E3779B1u + 0x7F4A7C15u;
        issue(2, 2); issue(3, 3);
        wait_vm<14>(); BAR();
        issue(4, 4); issue(5, 0);
        {
            const int ln = idx * 192 + (lw < 3 ? lw * 64 + lane : 0), row = ln / 12;
            int col = (xg & 1) * 768 + (ln - row * 12) * 64;
            col = col > FF - 2 ? FF - 2 : col;
            l2_touch(w1, (unsigned)(row * FF + col) * 2u, lds0 + L_SCR);
        }
        wait_vm<22>(); BAR(); issue(6, 1);
        wait_vm<22>(); BAR(); issue(7, 2);
        wait_vm<22>(); BAR(); issue(8, 3);
        wait_vm<22>(); BAR(); issue(9, 4);
        int slot = 0;
#pragma unroll 1
        for (int s = 5; s < 11; ++s) {
            wait_vm<21>(); BAR();
            issue(s + G_NSL, slot);
            slot = slot + 1 == G_NSL ? 0 : slot + 1;
        }
        wait_vm<21>(); BAR();
        wait_vm<14>(); BAR();
        wait_vm<7>(); BAR();
        wait_vm<0>(); BAR();
        if (lw == 3) {
            wait_flags(ws + WS_FQ + (head >> 1) * 256, 16, tag, lane);
            if (lane == 0) *(volatile lu32*)((lds_ptr)lds + C_QACQ) = 1u;
        }
        BAR();
        float kst, vst, qst;
        asm volatile("global_load_dword %0, %1, %2 sc1" : "=v"(kst) : "v"((unsigned)lane * 4u), "s"(ws + WS_KST + head * 256) : "memory");
        asm volatile("global_load_dword %0, %1, %2 sc1" : "=v"(vst) : "v"((unsigned)lane * 4u), "s"(ws + WS_VST + (head >> 1) * 256) : "memory");
        asm volatile("global_load_dword %0, %1, %2 sc1" : "=v"(qst) : "v"((unsigned)(lane & 7) * 4u), "s"(ws + WS_QST + head * 256 + qg * 32) : "memory");
        glds16c(ws + WS_Q + (size_t)(head * NTT + qg * 8 + lw * 2) * 1024, l16, lds0 + A_LQ + lw * 2048);
        glds16c(ws + WS_Q + (size_t)(head * NTT + qg * 8 + lw * 2 + 1) * 1024, l16, lds0 + A_LQ + lw * 2048 + 1024);
        const char* ksrc = ws + WS_K + (size_t)(head * NTT + lw) * 1024;
        const char* vsrc = ws + WS_V + (size_t)(head * NTT + lw) * 1024;
        auto kv = [&](int t) {
            glds16c(ksrc + t * 1024, l16, lds0 + (t + lw) * 2048);
            glds16c(vsrc + t * 1024, l16, lds0 + (t + lw) * 2048 + 1024);
        };
        auto kv_pub = [&](unsigned n) { if (lane == 0) *(volatile lu32*)((lds_ptr)lds + C_KV + lw * 4) = n; };
        kv(0); kv(4); kv(8); kv(12);
        asm volatile("s_waitcnt vmcnt(10)" : "+v"(kst), "+v"(vst), "+v"(qst) :: "memory");
        if (lw == 0) {
            const float kmax = dpp_max63(kst), vmax = dpp_max63(vst), qmax = dpp_max63(qst);
            if (lane == 63) *(volatile lu32*)((lds_ptr)lds + C_FAST) = (qmax * kmax * 1.01f <= A_THR * A_THR && vmax <= 1e18f) ? 1u : 0u;
            asm volatile("s_waitcnt lgkmcnt(0)" ::: "memory");
        }
        wait_vm<6>(); kv_pub(1);
        kv(16); kv(20); kv(24); kv(28);
        wait_vm<8>(); kv_pub(2);
        kv(32); kv(36); kv(40); kv(44);
        wait_vm<8>(); kv_pub(3);
        kv(48); kv(52); kv(56); kv(60);
#pragma unroll
        for (int j = 0; j < 2; ++j) {
            const int o = (lw * 2 + j) * 64 + lane;
            const int ln = idx * 472 + (o < 472 ? o : 0);
            l2_touch64(ln < 11008 ? (const char*)w2 + (size_t)ln * 128 : (const char*)wo + (size_t)(ln - 11008) * 128, lds0 + L_SCR);
        }
        wait_vm<10>(); kv_pub(4);
        wait_vm<2>(); kv_pub(5);
        __builtin_amdgcn_s_setprio(0);
    } else {
        const int tw = wid & 3, ns = wid >> 2;
        if (tid < 32) *(lu32*)((lds_ptr)lds + L_CTL + tid * 4) = 0u;
        if (wid == 11) {
            unsigned long long pc; unsigned len;
            asm volatile("s_getpc_b64 %0\n.Lcw_here:\n\ts_mov_b32 %1, .Lcw_end-.Lcw_here" : "=s"(pc), "=s"(len));
            for (unsigned o = 0; o < len; o += 8192) {
                const unsigned a = o + lane * 128;
                l2_touch((const void*)pc, a < len ? a : 0u, lds0 + L_SCR);
            }
        }
        tag = (unsigned)__builtin_amdgcn_readfirstlane(__hip_atomic_load((const gu32*)(ws + WS_EP), RLX_AGENT)) * 0x9E3779B1u + 0x7F4A7C15u;
        const int gtok = m0 + nt * 8 + (wid & 7);
        const u32x4 gx = *(const u32x4*)(x + (size_t)gtok * DM + lane * 8);
        const u32x4 gw = *(const u32x4*)(gate_w + lane * 8);
        float gs = 0.f;
#pragma unroll
        for (int q = 0; q < 4; ++q) { gs = fmaf(bflo(gx[q]), bflo(gw[q]), gs); gs = fmaf(bfhi(gx[q]), bfhi(gw[q]), gs); }
        gs = dpp_sum63(gs);
        PIN(gs);
        const int xrow = tw * 32 + r, xf7 = (xrow >> 1) & 7;
        int xa[4];
#pragma unroll
        for (int kk = 0; kk < 4; ++kk) xa[kk] = xrow * 128 + (((2 * kk + hi) ^ xf7) << 4);
        const int wa = G_XS + ((g4 >> 1) * 8 + (i16 >> 2)) * P_ROW + (ns * 32 + (g4 & 1) * 16 + (i16 & 3) * 4) * 2;
        f32x16 acc = f32x16{};
        GFrag fa, fb;
        LGKM_BAR();
        g_rd(fa, (lds_cptr)lds, xa, wa);
        TS(1);
        int so = G_STG;
#define GSTEP(FC, FN, ISV) { LGKM_BAR(); g_rd(FN, (lds_cptr)lds + so, xa, wa); so = so + G_STG == G_NSL * G_STG ? 0 : so + G_STG; g_mma<ISV>(acc, FC); }
        if (ns == 2) {
#pragma unroll 1
            for (int s = 0; s < 8; s += 2) { GSTEP(fa, fb, true) GSTEP(fb, fa, true) }
        } else {
#pragma unroll 1
            for (int s = 0; s < 8; s += 2) { GSTEP(fa, fb, false) GSTEP(fb, fa, false) }
        }
        TS(2);
        const int tt = mt * 4 + tw, h0 = nt * 2;
        if (ns < 2) {
            const float sc = ns == 0 ? QSCALE : 1.f;
            float ss[2];
#pragma unroll
            for (int e = 0; e < 2; ++e) {
                u32x4 p; float s2 = 0.f;
#pragma unroll
                for (int q = 0; q < 4; ++q) {
                    p[q] = cvtpk(acc[8 * e + 2 * q] * sc, acc[8 * e + 2 * q + 1] * sc);
                    const float a = bflo(p[q]), b = bfhi(p[q]);
                    s2 = fmaf(a, a, s2); s2 = fmaf(b, b, s2);
                }
                st16_sc1(ws + (ns == 0 ? WS_Q : WS_K) + (size_t)((h0 + e) * NTT + tt) * 1024, l16, p);
                ss[e] = s2;
            }
#pragma unroll
            for (int e = 0; e < 2; ++e) {
                ss[e] += xor32f(ss[e]);
                ss[e] = (ss[e] == ss[e]) ? ss[e] : __builtin_inff();
                ss[e] = dpp_max63(ss[e]);
            }
            if (lane == 63) {
                st4_sc1(ws + (ns == 0 ? WS_QST : WS_KST) + (size_t)(h0 * NTT + tt) * 4, ss[0]);
                st4_sc1(ws + (ns == 0 ? WS_QST : WS_KST) + (size_t)((h0 + 1) * NTT + tt) * 4, ss[1]);
            }
        } else {
            const unsigned vo = (unsigned)((r >> 4) * NTT * 1024 + hi * 256 + (r & 15) * 16);
            float vm = 0.f;
#pragma unroll
            for (int s2 = 0; s2 < 2; ++s2) {
                u32x4 pv;
#pragma unroll
                for (int q = 0; q < 4; ++q) {
                    const float a = acc[8 * s2 + 2 * q], b = acc[8 * s2 + 2 * q + 1];
                    pv[q] = cvtpk(a, b);
                    vm = fmaxf(vm, fmaxf((a == a) ? fabsf(a) : __builtin_inff(), (b == b) ? fabsf(b) : __builtin_inff()));
                }
                st16_sc1(ws + WS_V + (size_t)(h0 * NTT + tt) * 1024 + s2 * 512, vo, pv);
            }
            vm = dpp_max63(vm);
            if (lane == 63) st4_sc1(ws + WS_VST + (size_t)(nt * NTT + tt) * 4, vm);
        }
        if (wid < 8 && lane == 63) st4_sc1(ws + WS_G + (size_t)gtok * 4, 1.f / (1.f + __expf(-gs)));
        TS(3);
        acc = f32x16{};
        GSTEP(fa, fb, false)
        wait_vm<0>();
        GSTEP(fb, fa, false)
        raise_flag(ws + WS_FQ + nt * 256 + mt * 4, tag);
        GSTEP(fa, fb, false) GSTEP(fb, fa, false) GSTEP(fa, fb, false) GSTEP(fb, fa, false)
        u32x2 bia[2], bib[2];
        {
            int hi2 = tid; asm volatile("" : "+v"(hi2)); hi2 = (hi2 >> 5) & 1;
#pragma unroll
            for (int e = 0; e < 2; ++e) {
                int f = nt * P_BN + ns * 32 + 16 * e + 4 * hi2;
                f = f < FF ? f : 0;
                bia[e] = *(const u32x2*)(b1 + f);
                bib[e] = *(const u32x2*)(b1 + f + 8);
            }
        }
        GSTEP(fa, fb, false)
#undef GSTEP
        LGKM_BAR();
        if (wid == 0) {
#pragma unroll
            for (int j = 0; j < 2; ++j) {
                const unsigned one2 = ((lane & 15) == 0 || (lane & 15) == 4) ? 0x3f803f80u : 0u;
                *(__attribute__((address_space(3))) u32x4*)((lds_ptr)lds + A_LC + j * 1024 + lane * 16) = u32x4{one2, one2, one2, one2};
            }
            asm volatile("s_waitcnt lgkmcnt(0)" ::: "memory");
            if (lane == 0) *(volatile lu32*)((lds_ptr)lds + C_CST) = 1u;
        }
        g_mma<false>(acc, fb);
        TS(4);
#pragma unroll
        for (int e = 0; e < 2; ++e) {
            const int f0 = nt * P_BN + ns * 32 + 16 * e;
            const u32x2 ba = bia[e], bb = bib[e];
            u32x4 p;
            p[0] = cvtpk(mish_f(acc[8 * e + 0] + bflo(ba[0])), mish_f(acc[8 * e + 1] + bfhi(ba[0])));
            p[1] = cvtpk(mish_f(acc[8 * e + 2] + bflo(ba[1])), mish_f(acc[8 * e + 3] + bfhi(ba[1])));
            p[2] = cvtpk(mish_f(acc[8 * e + 4] + bflo(bb[0])), mish_f(acc[8 * e + 5] + bfhi(bb[0])));
            p[3] = cvtpk(mish_f(acc[8 * e + 6] + bflo(bb[1])), mish_f(acc[8 * e + 7] + bfhi(bb[1])));
            if (f0 < FF) st16_sc1(ws + WS_ACT + (size_t)(tt * FKS + (f0 >> 4)) * 1024, l16, p);
        }
    }
    auto act_done = [&]() {
        wait_vm<0>();
        if (lane == 0 && __hip_atomic_fetch_add((lu32*)((lds_ptr)lds + C_ACNT), 1u, RLX_WG) == 11u && nt < 15)
            __hip_atomic_store((gu32*)(ws + WS_FA + mt * 256 + nt * 4), tag, RLX_AGENT);
    };
    if (wid >= 4 && wid < 12) act_done();
    const int qp = wid & 3, kq = wid >> 2;
    {
        lds_wait_ge(lds, C_CST, 1u);
        lds_wait4_ge<2>(lds, C_KV, kq == 0 ? 1u : (unsigned)(kq + 2));
        TS(5);
        const bf16x8 qa = ldsfrag((lds_cptr)lds + A_LQ + qp * 2048 + lane * 16), qb = ldsfrag((lds_cptr)lds + A_LQ + qp * 2048 + 1024 + lane * 16);
        const bool fast = __builtin_amdgcn_readfirstlane(*(const lu32*)((lds_cptr)lds + C_FAST)) != 0;
        f32x16 oa = f32x16{}, ob = f32x16{};
        float ma = 0.f, mb = 0.f;
        const int kbase = kq * A_RG + lane * 16;
        const int vbase = r < 16 ? kq * A_RG + 1024 + hi * 256 + r * 16 : A_LC + hi * 256 + (r - 16) * 16;
        const int vinc = r < 16 ? 4096 : 0;
        const int voff1 = r < 16 ? 2048 : 1024;
        const f32x16 zero = f32x16{};
        if (fast) {
            lds_cptr kc = (lds_cptr)lds + kbase, vc = (lds_cptr)lds + vbase, vd = vc + voff1;
            bf16x8 kf = ldsfrag(kc);
            bf16x8 va0 = ldsfrag(vc), va1 = ldsfrag(vc + 512), vb0, vb1;
            f32x16 sa = MFMA(kf, qa, zero), sb = MFMA(kf, qb, zero);
            kf = ldsfrag(kc + 2048);
#define HALF(S, O, V0, V1)                                                              \
                _Pragma("unroll")                                                       \
                for (int i = 0; i < 8; ++i) S[i] = __builtin_amdgcn_exp2f(S[i]);        \
                O = MFMA(V0, pack8(S, 0), O);                                           \
                PIN(O); SBAR();                                                         \
                _Pragma("unroll")                                                       \
                for (int i = 8; i < 16; ++i) S[i] = __builtin_amdgcn_exp2f(S[i]);       \
                O = MFMA(V1, pack8(S, 1), O);                                           \
                PIN(O); SBAR();
#define TILE(VC0, VC1, VN0, VN1, KNEXT, VNEXT)                                          \
            {                                                                           \
                VN0 = ldsfrag(VNEXT); VN1 = ldsfrag((VNEXT) + 512);                     \
                SBAR();                                                                 \
                HALF(sa, oa, VC0, VC1)                                                  \
                sa = MFMA(kf, qa, zero); PIN(sa); SBAR();                               \
                HALF(sb, ob, VC0, VC1)                                                  \
                sb = MFMA(kf, qb, zero); PIN(sb);                                       \
                kf = ldsfrag(KNEXT);                                                    \
                SBAR();                                                                 \
            }
#pragma unroll 1
            for (int t = 0; t < 16; t += 2) {
                if (t == 2 && kq == 0) lds_wait4_ge(lds, C_KV, 2u);
                if (t == 6 && kq == 0) act_done();
                const int kn = t < 14 ? 4096 : 0;
                TILE(va0, va1, vb0, vb1, kc + kn, vd)
                vc += t < 14 ? vinc : 0;
                TILE(vb0, vb1, va0, va1, kc + kn + 2048, vc)
                kc += kn; vd += vinc;
            }
#undef TILE
#undef HALF
        } else {
            ma = -__builtin_inff(); mb = ma;
#pragma unroll 1
            for (int t = 0; t < 16; ++t) {
                if (t == 4 && kq == 0) { lds_wait4_ge(lds, C_KV, 2u); act_done(); }
                lds_cptr vc = (lds_cptr)lds + vbase + (r < 16 ? t * 2048 : 0);
                const bf16x8 kf = ldsfrag((lds_cptr)lds + kbase + t * 2048);
                const bf16x8 v0 = ldsfrag(vc), v1 = ldsfrag(vc + 512);
#define SLOW(Q, O, M)                                                                   \
                {                                                                       \
                    f32x16 s = MFMA(kf, Q, zero);                                       \
                    float mx = s[0];                                                    \
                    _Pragma("unroll")                                                   \
                    for (int i = 1; i < 16; ++i) mx = fmaxf(mx, s[i]);                  \
                    mx = fmaxf(mx, xor32f(mx));                                         \
                    const float mn = fmaxf(M, mx);                                      \
                    const float al = __builtin_amdgcn_exp2f(M - mn);                    \
                    M = mn;                                                             \
                    _Pragma("unroll")                                                   \
                    for (int i = 0; i < 16; ++i) { s[i] = __builtin_amdgcn_exp2f(s[i] - mn); O[i] *= al; } \
                    O = MFMA(v0, pack8(s, 0), O);                                       \
                    O = MFMA(v1, pack8(s, 1), O);                                       \
                }
                SLOW(qa, oa, ma)
                SLOW(qb, ob, mb)
#undef SLOW
            }
        }
        TS(6);
        {
            int tz = tid; asm volatile("" : "+v"(tz));
            lds_ptr ch = (lds_ptr)lds + A_CHAIN + qp * 5120 + (tz & 63) * 16;
            lds_ptr cl = (lds_ptr)lds + A_CHAIN + qp * 5120 + 4096 + (tz & 63) * 16;
            if (kq > 0) {
                lds_wait_ge(lds, C_CHAIN + qp * 4, (unsigned)kq);
                const f32x4 a0 = *(__attribute__((address_space(3))) f32x4*)(ch), a1 = *(__attribute__((address_space(3))) f32x4*)(ch + 1024);
                const f32x4 b0 = *(__attribute__((address_space(3))) f32x4*)(ch + 2048), b1v = *(__attribute__((address_space(3))) f32x4*)(ch + 3072);
                const f32x4 lm = *(__attribute__((address_space(3))) f32x4*)(cl);
                const float na = fmaxf(ma, lm[1]), nb = fmaxf(mb, lm[3]);
                const float fa = __builtin_amdgcn_exp2f(ma - na), xa2 = __builtin_amdgcn_exp2f(lm[1] - na);
                const float fb = __builtin_amdgcn_exp2f(mb - nb), xb2 = __builtin_amdgcn_exp2f(lm[3] - nb);
#pragma unroll
                for (int e = 0; e < 4; ++e) {
                    oa[e] = fmaf(a0[e], xa2, oa[e] * fa); oa[4 + e] = fmaf(a1[e], xa2, oa[4 + e] * fa);
                    ob[e] = fmaf(b0[e], xb2, ob[e] * fb); ob[4 + e] = fmaf(b1v[e], xb2, ob[4 + e] * fb);
                }
                oa[8] = fmaf(lm[0], xa2, oa[8] * fa); ob[8] = fmaf(lm[2], xb2, ob[8] * fb);
                ma = na; mb = nb;
            }
            if (kq < 3) {
                *(__attribute__((address_space(3))) f32x4*)(ch) = f32x4{oa[0], oa[1], oa[2], oa[3]};
                *(__attribute__((address_space(3))) f32x4*)(ch + 1024) = f32x4{oa[4], oa[5], oa[6], oa[7]};
                *(__attribute__((address_space(3))) f32x4*)(ch + 2048) = f32x4{ob[0], ob[1], ob[2], ob[3]};
                *(__attribute__((address_space(3))) f32x4*)(ch + 3072) = f32x4{ob[4], ob[5], ob[6], ob[7]};
                *(__attribute__((address_space(3))) f32x4*)(cl) = f32x4{oa[8], ma, ob[8], mb};
                lds_add1(lds, C_CHAIN + qp * 4, lane);
            } else {
                const float ra = 1.f / oa[8], rb = 1.f / ob[8];
                u32x4 pa, pb;
#pragma unroll
                for (int q = 0; q < 4; ++q) { pa[q] = cvtpk(oa[2 * q] * ra, oa[2 * q + 1] * ra); pb[q] = cvtpk(ob[2 * q] * rb, ob[2 * q + 1] * rb); }
                const int qt = qg * 8 + qp * 2;
                st16_sc1(ws + WS_O + (size_t)(qt * NH + head) * 1024, l16, pa);
                st16_sc1(ws + WS_O + (size_t)((qt + 1) * NH + head) * 1024, l16, pb);
                wait_vm<0>();
            }
            if (kq >= 2) {
                unsigned prev = 0;
                lu32* cnt = (lu32*)((lds_ptr)lds + C_OCNT) + (kq == 2 ? (C_G2 - C_OCNT) / 4 : 0);
                if (lane == 0) prev = __hip_atomic_fetch_add(cnt, 1u, RLX_WG);
                if (__builtin_amdgcn_readfirstlane(prev) == 3) {
                    if (kq == 3 && lane == 0) __hip_atomic_store((gu32*)(ws + WS_FO + qg * 256 + head * 4), tag, RLX_AGENT);
                    const unsigned wo_l = (unsigned)(((kq - 2) * 256 + ((tz >> 5) & 1) * 4 + ((tz >> 2) & 3)) * DM + nt4 * 64 + ((tz >> 4) & 1) * 32 + (tz & 3) * 8) * 2u;
#pragma unroll 1
                    for (int pc = 0; pc < 32; pc += 4) {
#pragma unroll
                        for (int j = 0; j < 4; ++j) glds16(wo + (size_t)(pc + j) * 8 * DM, wo_l, lds0 + kq * A_RG + (pc + j) * 1024);
                    }
                    wait_vm<0>();
                    lds_add1(lds, C_WOR, lane);
                }
            }
        }
        TS(7);
    }
    if (kq >= 2) return;
    {
        int tz = tid; asm volatile("" : "+v"(tz));
        const unsigned lz16 = (tz & 63) * 16;
        if (kq == 0) {
            const char* aF = ws + WS_ACT + (size_t)(mt4 * 2 * FKS + qp) * 1024;
            const unsigned wo_l = (unsigned)((qp * 8 + ((tz >> 5) & 1) * 4 + ((tz >> 2) & 3)) * DM + nt4 * 64 + ((tz >> 4) & 1) * 32 + (tz & 3) * 8) * 2u;
            const unsigned lb = lds0 + M_BASE + qp * 1024;
            auto issue = [&](const char* a, const u16* w, int wadj, int s) {
                const unsigned base = lb + (s & 3) * M_STG;
                glds16c(a, lz16, base);
                glds16c(a + (size_t)FKS * 1024, lz16, base + 4096);
                glds16(w, wo_l, base + 8192);
                glds16(w + wadj * DM, wo_l, base + 12288);
            };
            auto publish_full = [&](int n) { if (lane == 0) *(volatile lu32*)((lds_ptr)lds + C_FULL + qp * 4) = (unsigned)n; };
            if (qp == 0) {
                wait_flags(ws + WS_FA + (mt4 >> 1) * 256, 15, tag, lane);
                if (lane == 0) *(volatile lu32*)((lds_ptr)lds + C_OACQ) = 1u;
#pragma unroll
                for (int j = 0; j < 3; ++j) {
                    const int o = j * 64 + lane;
                    l2_touch(ws + WS_ACT + (size_t)mt4 * 2 * FKS * 1024, (unsigned)(nt4 * 172 + (o < 172 ? o : 0)) * 128u, lds0 + L_SCR);
                }
            } else {
                lds_wait_ge(lds, C_OACQ, 1u);
            }
            lds_wait4_ge<4>(lds, C_CHAIN, 1u);
            issue(aF, w2, 32, 0); issue(aF + 4096, w2 + (size_t)64 * DM, 32, 1);
            wait_vm<0>(); publish_full(2);
            lds_wait4_ge<8>(lds, C_CHAIN, 2u);
            issue(aF + 2 * 4096, w2 + (size_t)2 * 64 * DM, 32, 2); issue(aF + 3 * 4096, w2 + (size_t)3 * 64 * DM, 32, 3);
            TS(8);
#pragma unroll 1
            for (int s = 4; s < M_NS1; ++s) {
                wait_vm<8>(); publish_full(s - 2);
                lds_wait4_ge(lds, C_FREE, (unsigned)(s - 3));
                const bool lastst = s == M_NS1 - 1;
                issue(aF + (size_t)s * 4096 - ((lastst && (qp & 2)) ? 2048 : 0), w2 + (size_t)s * 64 * DM, lastst ? 0 : 32, s);
            }
            wait_vm<8>(); publish_full(M_NS1 - 2);
            wait_vm<4>(); publish_full(M_NS1 - 1);
            wait_vm<0>(); publish_full(M_NS1);
            TS(11);
            if (qp == 0) {
                wait_flags(ws + WS_FO + (mt4 >> 2) * 256, 32, tag, lane);
                if (lane == 0) *(volatile lu32*)((lds_ptr)lds + C_OACQ) = 2u;
            } else {
                lds_wait_ge(lds, C_OACQ, 2u);
            }
            const char* aO = ws + WS_O + (size_t)(mt4 * 2 * NH + qp) * 1024;
#pragma unroll
            for (int j = 0; j < 4; ++j) {
                const int s = M_NS1 + j;
                lds_wait4_ge(lds, C_FREE, (unsigned)(s - 3));
                const unsigned base = lb + (s & 3) * M_STG;
                glds16c(aO + (size_t)j * 8192, lz16, base);
                glds16c(aO + (size_t)j * 8192 + 4096, lz16, base + 4096);
                glds16c(aO + (size_t)(NH + j * 8) * 1024, lz16, base + 8192);
                glds16c(aO + (size_t)(NH + j * 8) * 1024 + 4096, lz16, base + 12288);
            }
            wait_vm<12>(); publish_full(M_NS1 + 1);
            wait_vm<8>(); publish_full(M_NS1 + 2);
            wait_vm<4>(); publish_full(M_NS1 + 3);
            wait_vm<0>(); publish_full(M_NS1 + 4);
        } else {
            const int wm = qp & 1, wn = qp >> 1;
            const int wl = (((tz >> 5) & 1) * 2 + wn) * 256 + ((tz >> 2) & 3) * 64 + ((tz >> 4) & 1) * 32 + (tz & 3) * 8;
            lds_cptr W = (lds_cptr)lds + M_BASE + 8192 + wl;
            lds_cptr A = (lds_cptr)lds + M_BASE + wm * 4096 + lz16;
            f32x16 acca = f32x16{}, accb = f32x16{};
            MFrag4 fa, fb;
            auto release = [&](int n) {
                asm volatile("s_waitcnt lgkmcnt(0)" ::: "memory");
                if (lane == 0) *(volatile lu32*)((lds_ptr)lds + C_FREE + qp * 4) = (unsigned)n;
            };
            lds_wait4_ge(lds, C_FULL, 1u);
            m_rd4(fa, A, W);
            release(1);
#define MIT(S, FC, FN, NK)                                                                                               \
            {                                                                                                            \
                if ((S) + 1 < M_NS1) {                                                                                   \
                    lds_wait4_ge(lds, C_FULL, (unsigned)((S) + 2));                                                      \
                    m_rd4(FN, A + (((S) + 1) & 3) * M_STG, W + (((S) + 1) & 3) * M_STG);                                 \
                }                                                                                                        \
                SBAR();                                                                                                  \
                _Pragma("unroll")                                                                                        \
                for (int kk = 0; kk < NK; ++kk) accb = MFMA(FC.w[kk], FC.a[kk], accb);                                   \
                PIN(accb);                                                                                               \
                SBAR();                                                                                                  \
                if ((S) + 1 < M_NS1) release((S) + 2);                                                                   \
            }
#pragma unroll 1
            for (int s = 0; s < M_NS1 - 2; s += 2) { MIT(s, fa, fb, 4) MIT(s + 1, fb, fa, 4) }
            MIT(M_NS1 - 2, fa, fb, 4) MIT(M_NS1 - 1, fb, fa, 2)
#undef MIT
            lds_wait_ge(lds, C_WOR, 2u);
            float g = 0.f;
            lds_cptr AO = (lds_cptr)lds + M_BASE + wm * 8192 + lz16;
#pragma unroll
            for (int j = 0; j < 4; ++j) {
                lds_wait4_ge(lds, C_FULL, (unsigned)(M_NS1 + j + 1));
                if (j == 0) asm volatile("global_load_dword %0, %1, %2 sc1" : "=v"(g) : "v"((unsigned)(mt4 * 64 + wm * 32 + (tz & 31)) * 4u), "s"(ws + WS_G) : "memory");
                lds_cptr Wj = (lds_cptr)lds + 2 * A_RG + j * 16384 + wl;
                m_rd4(fa, AO + ((M_NS1 + j) & 3) * M_STG, Wj);
                m_rd4(fb, AO + ((M_NS1 + j) & 3) * M_STG + 4096, Wj + 8192);
#pragma unroll
                for (int kk = 0; kk < 4; ++kk) acca = MFMA(fa.w[kk], fa.a[kk], acca);
#pragma unroll
                for (int kk = 0; kk < 4; ++kk) acca = MFMA(fb.w[kk], fb.a[kk], acca);
            }
            asm volatile("s_waitcnt vmcnt(0)" : "+v"(g) :: "memory");
            TS(9);
            const float g1 = 1.f - g;
            u16* orow = out + (size_t)(mt4 * 64 + wm * 32 + (tz & 31)) * DM + nt4 * 64 + wn * 32 + ((tz >> 5) & 1) * 4;
#pragma unroll
            for (int q = 0; q < 4; ++q) {
                u32x2 p;
                p[0] = cvtpk(g * acca[4 * q] + g1 * accb[4 * q], g * acca[4 * q + 1] + g1 * accb[4 * q + 1]);
                p[1] = cvtpk(g * acca[4 * q + 2] + g1 * accb[4 * q + 2], g * acca[4 * q + 3] + g1 * accb[4 * q + 3]);
                *(u32x2*)(orow + q * 8) = p;
            }
        }
    }
    if (blockIdx.x == 0 && tid == 0) __hip_atomic_store((gu32*)(ws + WS_EP), tag, RLX_AGENT);
    TS(10);
    asm volatile(".Lcw_end:");
}

extern "C" void kernel_launch(void* const* d_in, const int* in_sizes, int n_in, void* d_out, int out_size, void* d_ws, size_t ws_size,
                              hipStream_t stream) {
    (void)hipFuncSetAttribute((const void*)fused_kernel, hipFuncAttributeMaxDynamicSharedMemorySize, LDS_BYTES);
    fused_kernel<<<256, 1024, LDS_BYTES, stream>>>((const u16*)d_in[0], (const u16*)d_in[1], (const u16*)d_in[2], (const u16*)d_in[4],
                                                   (const u16*)d_in[5], (const u16*)d_in[6], (const u16*)d_in[7], (char*)d_ws, (u16*)d_out);
}
```

```cpp
#include <hip/hip_runtime.h>
#include <hip/hip_bf16.h>
#include <stdint.h>

using bf16x8 = __attribute__((ext_vector_type(8))) short;
using s16x4  = __attribute__((ext_vector_type(4))) short;
using f32x16 = __attribute__((ext_vector_type(16))) float;
using f32x4  = __attribute__((ext_vector_type(4))) float;
using f32x2  = __attribute__((ext_vector_type(2))) float;
using u32x4  = __attribute__((ext_vector_type(4))) unsigned;
using u32x2  = __attribute__((ext_vector_type(2))) unsigned;
typedef __bf16 bf16x2_t __attribute__((ext_vector_type(2)));
typedef short v4i16_t __attribute__((ext_vector_type(4)));
typedef __attribute__((address_space(3))) const char* lds_cptr;
typedef __attribute__((address_space(3))) char* lds_ptr;
typedef unsigned short u16;

#define DI __device__ __forceinline__
#define TS(i)
#define TSEND(k)
#define MFMA(a, b, c) __builtin_amdgcn_mfma_f32_32x32x16_bf16((a), (b), (c), 0, 0, 0)
#define SBAR() __builtin_amdgcn_sched_barrier(0)
#define PIN(x) asm volatile("" : "+v"(x))

constexpr int SEQ = 2048, DM = 512, NH = 32, DH = 16, FF = 1376, NQKV = 1536;
constexpr int NTT = SEQ / 32;
constexpr int FKS = FF / 16;
constexpr float QSCALE = 0.25f * 1.4426950408889634f;

constexpr size_t WS_Q = 0;
constexpr size_t WS_K = WS_Q + (size_t)SEQ * DM * 2;
constexpr size_t WS_V = WS_K + (size_t)SEQ * DM * 2;
constexpr size_t WS_O = WS_V + (size_t)SEQ * DM * 2;
constexpr size_t WS_ACT = WS_O + (size_t)SEQ * DM * 2;
constexpr size_t WS_KST = WS_ACT + (size_t)SEQ * FF * 2;
constexpr size_t WS_VST = WS_KST + (size_t)NH * NTT * 4;
constexpr size_t WS_QST = WS_VST + (size_t)16 * NTT * 4;
constexpr size_t WS_G = WS_QST + (size_t)NH * NTT * 4;
constexpr size_t WS_FQ = WS_G + (size_t)SEQ * 4;
constexpr size_t WS_FA = WS_FQ + 16 * 256;
constexpr size_t WS_FO = WS_FA + 16 * 256;
constexpr size_t WS_EP = WS_FO + 8 * 256;
constexpr size_t WS_SCR = WS_EP + 256;

DI unsigned cvtpk(float lo, float hi) { f32x2 v = {lo, hi}; return __builtin_bit_cast(unsigned, __builtin_convertvector(v, bf16x2_t)); }
DI float bf2f(u16 v) { return __uint_as_float((unsigned)v << 16); }
DI float bflo(unsigned v) { return __uint_as_float(v << 16); }
DI float bfhi(unsigned v) { return __uint_as_float(v & 0xffff0000u); }
DI void glds16(const void* sbase, unsigned voff, unsigned lds_base) {
    unsigned sv;
    asm volatile("s_mov_b32 %0, m0\n\ts_mov_b32 m0, %3\n\ts_nop 0\n\tglobal_load_lds_dwordx4 %1, %2\n\ts_mov_b32 m0, %0"
                 : "=&s"(sv) : "v"(voff), "s"(sbase), "s"(lds_base) : "memory");
}
DI void glds16c(const void* sbase, unsigned voff, unsigned lds_base) {
    unsigned sv;
    asm volatile("s_mov_b32 %0, m0\n\ts_mov_b32 m0, %3\n\ts_nop 0\n\tglobal_load_lds_dwordx4 %1, %2 sc1\n\ts_mov_b32 m0, %0"
                 : "=&s"(sv) : "v"(voff), "s"(sbase), "s"(lds_base) : "memory");
}
template <int OFF> DI void gload16(bf16x8& dst, const void* sbase, unsigned voff) {
    asm volatile("global_load_dwordx4 %0, %1, %2 offset:%3" : "=v"(dst) : "v"(voff), "s"(sbase), "i"(OFF) : "memory");
}
DI s16x4 trread(lds_cptr p) { return __builtin_bit_cast(s16x4, __builtin_amdgcn_ds_read_tr16_b64_v4i16((__attribute__((address_space(3))) v4i16_t*)p)); }
DI bf16x8 ldsfrag(lds_cptr p) { return *(const __attribute__((address_space(3))) bf16x8*)p; }
DI bf16x8 cat8(s16x4 a, s16x4 b) { return __builtin_shufflevector(a, b, 0, 1, 2, 3, 4, 5, 6, 7); }
template <int N> DI void wait_vm_bar() { asm volatile("s_waitcnt vmcnt(%0)\n\ts_barrier" :: "i"(N) : "memory"); }
template <int N> DI void wait_vm_lgkm_bar() { asm volatile("s_waitcnt vmcnt(%0) lgkmcnt(0)\n\ts_barrier" :: "i"(N) : "memory"); }
DI float xor32f(float v) {
    auto r = __builtin_amdgcn_permlane32_swap(__float_as_uint(v), __float_as_uint(v), false, false);
    return (threadIdx.x & 32) ? __uint_as_float(r[0]) : __uint_as_float(r[1]);
}
DI float dpp_max63(float v) {
#define DSTEP(ctrl, rmask) v = fmaxf(v, __int_as_float(__builtin_amdgcn_update_dpp(__float_as_int(v), __float_as_int(v), ctrl, rmask, 0xf, false)))
    DSTEP(0x111, 0xf); DSTEP(0x112, 0xf); DSTEP(0x114, 0xf); DSTEP(0x118, 0xf); DSTEP(0x142, 0xa); DSTEP(0x143, 0xc);
#undef DSTEP
    return v;
}
DI float dpp_sum63(float v) {
#define DSTEP(ctrl, rmask) v += __int_as_float(__builtin_amdgcn_update_dpp(0, __float_as_int(v), ctrl, rmask, 0xf, false))
    DSTEP(0x111, 0xf); DSTEP(0x112, 0xf); DSTEP(0x114, 0xf); DSTEP(0x118, 0xf); DSTEP(0x142, 0xa); DSTEP(0x143, 0xc);
#undef DSTEP
    return v;
}
DI float bcast63(float v) { return __int_as_float(__builtin_amdgcn_readlane(__float_as_int(v), 63)); }
DI float mish_f(float h) {
    float t = __builtin_amdgcn_exp2f(-fabsf(h) * 1.4426950408889634f);
    float t2 = t * t;
    float num = h > 0.f ? 1.f + 2.f * t : t2 + 2.f * t;
    float den = h > 0.f ? num + 2.f * t2 : num + 2.f;
    return h * num * __builtin_amdgcn_rcpf(den);
}

typedef __attribute__((address_space(1))) unsigned gu32;
#define RLX_AGENT __ATOMIC_RELAXED, __HIP_MEMORY_SCOPE_AGENT
DI void st16_sc1(void* sbase, unsigned voff, u32x4 v) {
    asm volatile("global_store_dwordx4 %0, %1, %2 sc1\n\ts_nop 1" :: "v"(voff), "v"(v), "s"(sbase) : "memory");
}
DI void st4_sc1(void* p, float v) { __hip_atomic_store((gu32*)p, __float_as_uint(v), RLX_AGENT); }
DI void publish(void* flag, unsigned tag) {
    asm volatile("s_waitcnt vmcnt(0) lgkmcnt(0)\n\ts_barrier" ::: "memory");
    if (threadIdx.x == 0) __hip_atomic_store((gu32*)flag, tag, RLX_AGENT);
}
DI void wait_flags(const void* flags, int n, unsigned tag, int lane) {
    for (unsigned spins = 0; spins < (1u << 18); ++spins) {
        unsigned v = tag;
        if (lane < n) v = __hip_atomic_load((const gu32*)flags + lane, RLX_AGENT);
        if (__all(v == tag)) break;
        __builtin_amdgcn_s_sleep(2);
    }
    __builtin_amdgcn_fence(__ATOMIC_ACQUIRE, "wavefront");
    asm volatile("" ::: "memory");
}

constexpr int P_BN = 96, P_ROW = P_BN * 2;
constexpr int G_XS = 16384, G_STG = G_XS + 64 * P_ROW, G_NSL = 5, LDS_BYTES = 163840;
constexpr int A_RG = 32768, A_LC = 4 * A_RG, A_CHAIN = A_LC + 2048;
constexpr int A_LQ = A_CHAIN + 20480, L_CTL = A_LQ + 8192;
constexpr int C_FAST = L_CTL, C_OACQ = L_CTL + 4, C_OCNT = L_CTL + 8, C_WOR = L_CTL + 12, C_CHAIN = L_CTL + 16, C_FULL = L_CTL + 32, C_FREE = L_CTL + 48, C_KV = L_CTL + 64, C_G2 = L_CTL + 80, C_QACQ = L_CTL + 84, C_ACNT = L_CTL + 88, C_CST = L_CTL + 92;
constexpr int M_STG = 32768, M_NS1 = (FF + 127) / 128, M_NS = M_NS1 + DM / 128;
constexpr float A_THR = 40.f;
static_assert(L_CTL >= G_NSL * G_STG && L_CTL + 128 <= LDS_BYTES - 256, "lds map");
typedef __attribute__((address_space(3))) unsigned lu32;
#define RLX_WG __ATOMIC_RELAXED, __HIP_MEMORY_SCOPE_WORKGROUP

DI bf16x8 pack8(const f32x16& p, int s) {
    u32x4 r;
#pragma unroll
    for (int q = 0; q < 4; ++q) r[q] = cvtpk(p[8 * s + 2 * q], p[8 * s + 2 * q + 1]);
    return __builtin_bit_cast(bf16x8, r);
}
#define LGKM_BAR() asm volatile("s_waitcnt lgkmcnt(0)\n\ts_barrier" ::: "memory")
#define BAR() asm volatile("s_barrier" ::: "memory")
template <int N> DI void wait_vm() { asm volatile("s_waitcnt vmcnt(%0)" :: "i"(N) : "memory"); }
DI void raise_flag(void* flag, unsigned tag) { if (threadIdx.x == 0) __hip_atomic_store((gu32*)flag, tag, RLX_AGENT); }
DI void lds_wait_ge(char* lds, int off, unsigned want) {
    asm volatile("" ::: "memory");
    while ((unsigned)__builtin_amdgcn_readfirstlane(__hip_atomic_load((lu32*)((lds_ptr)lds + off), RLX_WG)) < want) __builtin_amdgcn_s_sleep(4);
    asm volatile("" ::: "memory");
}
template <int SLEEP = 1>
DI void lds_wait4_ge(char* lds, int off, unsigned want) {
    asm volatile("" ::: "memory");
    for (;;) {
        const u32x4 v = *(volatile __attribute__((address_space(3))) u32x4*)((lds_ptr)lds + off);
        const unsigned m = min(min(v[0], v[1]), min(v[2], v[3]));
        if ((unsigned)__builtin_amdgcn_readfirstlane(m) >= want) break;
        __builtin_amdgcn_s_sleep(SLEEP);
    }
    asm volatile("" ::: "memory");
}
DI void lds_add1(char* lds, int off, int lane) {
    asm volatile("s_waitcnt lgkmcnt(0)" ::: "memory");
    if (lane == 0) __hip_atomic_fetch_add((lu32*)((lds_ptr)lds + off), 1u, RLX_WG);
}
DI bool try_flags(const void* flags, int n, unsigned tag, int lane) {
    unsigned v = tag;
    if (lane < n) v = __hip_atomic_load((const gu32*)flags + lane, RLX_AGENT);
    if (!__all(v == tag)) return false;
    __builtin_amdgcn_fence(__ATOMIC_ACQUIRE, "agent");
    return true;
}

DI void l2_touch(const void* sbase, unsigned voff, unsigned lds_scratch) {
    unsigned sv;
    asm volatile("s_mov_b32 %0, m0\n\ts_mov_b32 m0, %3\n\ts_nop 0\n\tglobal_load_lds_dword %1, %2\n\ts_mov_b32 m0, %0"
                 : "=&s"(sv) : "v"(voff), "s"(sbase), "s"(lds_scratch) : "memory");
}
DI void l2_touch64(const void* p, unsigned lds_scratch) {
    unsigned sv;
    asm volatile("s_mov_b32 %0, m0\n\ts_mov_b32 m0, %2\n\ts_nop 0\n\tglobal_load_lds_dword %1, off\n\ts_mov_b32 m0, %0"
                 : "=&s"(sv) : "v"(p), "s"(lds_scratch) : "memory");
}
constexpr int L_SCR = 163840 - 256;
struct GFrag { bf16x8 x[4]; bf16x8 w[4]; };
DI void g_rd(GFrag& f, lds_cptr b, const int (&xa)[4], int wa) {
#pragma unroll
    for (int kk = 0; kk < 4; ++kk) {
        f.x[kk] = ldsfrag(b + xa[kk]);
        f.w[kk] = cat8(trread(b + wa + kk * 16 * P_ROW), trread(b + wa + (kk * 16 + 4) * P_ROW));
    }
}
template <bool ISV> DI void g_mma(f32x16& acc, const GFrag& f) {
    SBAR();
#pragma unroll
    for (int kk = 0; kk < 4; ++kk) { if (ISV) acc = MFMA(f.x[kk], f.w[kk], acc); else acc = MFMA(f.w[kk], f.x[kk], acc); }
    PIN(acc);
    SBAR();
}
struct MFrag { bf16x8 a0, a1, w0, w1; };
DI void m_rd(MFrag& f, lds_cptr A, lds_cptr W) {
    f.w0 = cat8(trread(W), trread(W + 1024)); f.a0 = ldsfrag(A);
    f.w1 = cat8(trread(W + 2048), trread(W + 3072)); f.a1 = ldsfrag(A + 1024);
}

__global__ __launch_bounds__(1024, 4) void fused_kernel(const u16* __restrict__ x, const u16* __restrict__ wqkv, const u16* __restrict__ wo,
                                                        const u16* __restrict__ w1, const u16* __restrict__ b1, const u16* __restrict__ w2,
                                                        const u16* __restrict__ gate_w, char* __restrict__ ws, u16* __restrict__ out) {
    extern __shared__ __attribute__((aligned(16))) char lds[];
    const int tid = threadIdx.x, lane = tid & 63, wid = __builtin_amdgcn_readfirstlane(tid >> 6);
    const int r = lane & 31, hi = lane >> 5;
    const int xg = blockIdx.x & 7, idx = blockIdx.x >> 3;
    const unsigned lds0 = (unsigned)(uintptr_t)lds;
    const unsigned l16 = lane * 16;
    const int i16 = lane & 15, g4 = lane >> 4;
    TS(0);
    const int mt = (xg >> 1) * 4 + (idx & 3), nt = (xg & 1) * 8 + (idx >> 2);
    const int head = xg * 4 + (idx & 3), qg = idx >> 2;
    const int mt4 = xg * 4 + (idx & 3), nt4 = idx >> 2;
    const int m0 = mt * 128;
    unsigned tag;

    if (wid >= 12) {
        const int lw = wid - 12;
        __builtin_amdgcn_s_setprio(3);
        const int rot = ((idx >> 2) + 2 * (idx & 3)) & 7;
        unsigned epv;
        asm volatile("global_load_dword %0, %1, %2 sc1" : "=v"(epv) : "v"(0u), "s"(ws + WS_EP) : "memory");
        unsigned woq[3], wof[3];
#pragma unroll
        for (int j = 0; j < 3; ++j) {
            const int o = (lw * 3 + j) * 1024 + lane * 16;
            const int k = o / P_ROW, cc = (o % P_ROW) >> 4;
            woq[j] = (unsigned)(k * NQKV + (cc >> 2) * 512 + nt * 32 + (cc & 3) * 8) * 2u;
            int n = nt * P_BN + cc * 8;
            n = n > FF - 8 ? FF - 8 : n;
            wof[j] = (unsigned)(k * FF + n) * 2u;
        }
        const unsigned xoff = (unsigned)((m0 + lw * 32 + (lane >> 3)) * DM) * 2u + (((lane & 7) ^ g4) << 4), xoff1 = xoff ^ 64u;
        auto issue = [&](int s, int slot) {
            const int kb = (s + rot) & 7;
            const unsigned base = lds0 + slot * G_STG;
            const u16* xs = x + kb * 64;
            glds16(xs, xoff, base + lw * 4096);
            glds16(xs + 8 * DM, xoff1, base + lw * 4096 + 1024);
            glds16(xs + 16 * DM, xoff, base + lw * 4096 + 2048);
            glds16(xs + 24 * DM, xoff1, base + lw * 4096 + 3072);
            const u16* wsrc = s < 8 ? wqkv + (size_t)kb * 64 * NQKV : w1 + (size_t)kb * 64 * FF;
#pragma unroll
            for (int j = 0; j < 3; ++j) glds16(wsrc, s < 8 ? woq[j] : wof[j], base + G_XS + (lw * 3 + j) * 1024);
        };
        issue(0, 0); issue(1, 1);
        asm volatile("s_waitcnt vmcnt(7)\n\ts_barrier" : "+v"(epv) :: "memory");
        tag = (unsigned)__builtin_amdgcn_readfirstlane(epv) * 0x9E3779B1u + 0x7F4A7C15u;
        issue(2, 2); issue(3, 3);
        wait_vm<14>(); BAR();
        issue(4, 4); issue(5, 0);
        {
            const int ln = idx * 192 + (lw < 3 ? lw * 64 + lane : 0), row = ln / 12;
            int col = (xg & 1) * 768 + (ln - row * 12) * 64;
            col = col > FF - 2 ? FF - 2 : col;
            l2_touch(w1, (unsigned)(row * FF + col) * 2u, lds0 + L_SCR);
        }
        wait_vm<22>(); BAR(); issue(6, 1);
        wait_vm<22>(); BAR(); issue(7, 2);
        wait_vm<22>(); BAR(); issue(8, 3);
        wait_vm<22>(); BAR(); issue(9, 4);
        int slot = 0;
#pragma unroll 1
        for (int s = 5; s < 11; ++s) {
            wait_vm<21>(); BAR();
            issue(s + G_NSL, slot);
            slot = slot + 1 == G_NSL ? 0 : slot + 1;
        }
        wait_vm<21>(); BAR();
        wait_vm<14>(); BAR();
        wait_vm<7>(); BAR();
        wait_vm<0>(); BAR();
        if (lw == 3) {
            wait_flags(ws + WS_FQ + (head >> 1) * 256, 16, tag, lane);
            if (lane == 0) *(volatile lu32*)((lds_ptr)lds + C_QACQ) = 1u;
        }
        BAR();
        float kst, vst, qst;
        asm volatile("global_load_dword %0, %1, %2 sc1" : "=v"(kst) : "v"((unsigned)lane * 4u), "s"(ws + WS_KST + head * 256) : "memory");
        asm volatile("global_load_dword %0, %1, %2 sc1" : "=v"(vst) : "v"((unsigned)lane * 4u), "s"(ws + WS_VST + (head >> 1) * 256) : "memory");
        asm volatile("global_load_dword %0, %1, %2 sc1" : "=v"(qst) : "v"((unsigned)(lane & 7) * 4u), "s"(ws + WS_QST + head * 256 + qg * 32) : "memory");
        glds16c(ws + WS_Q + (size_t)(head * NTT + qg * 8 + lw * 2) * 1024, l16, lds0 + A_LQ + lw * 2048);
        glds16c(ws + WS_Q + (size_t)(head * NTT + qg * 8 + lw * 2 + 1) * 1024, l16, lds0 + A_LQ + lw * 2048 + 1024);
        const char* ksrc = ws + WS_K + (size_t)(head * NTT + lw) * 1024;
        const char* vsrc = ws + WS_V + (size_t)(head * NTT + lw) * 1024;
        auto kv = [&](int t) {
            glds16c(ksrc + t * 1024, l16, lds0 + (t + lw) * 2048);
            glds16c(vsrc + t * 1024, l16, lds0 + (t + lw) * 2048 + 1024);
        };
        auto kv_pub = [&](unsigned n) { if (lane == 0) *(volatile lu32*)((lds_ptr)lds + C_KV + lw * 4) = n; };
        kv(0); kv(4); kv(8); kv(12);
        asm volatile("s_waitcnt vmcnt(10)" : "+v"(kst), "+v"(vst), "+v"(qst) :: "memory");
        if (lw == 0) {
            const float kmax = dpp_max63(kst), vmax = dpp_max63(vst), qmax = dpp_max63(qst);
            if (lane == 63) *(volatile lu32*)((lds_ptr)lds + C_FAST) = (qmax * kmax * 1.01f <= A_THR * A_THR && vmax <= 1e18f) ? 1u : 0u;
            asm volatile("s_waitcnt lgkmcnt(0)" ::: "memory");
        }
        wait_vm<6>(); kv_pub(1);
        kv(16); kv(20); kv(24); kv(28);
        wait_vm<8>(); kv_pub(2);
        kv(32); kv(36); kv(40); kv(44);
        wait_vm<8>(); kv_pub(3);
        kv(48); kv(52); kv(56); kv(60);
#pragma unroll
        for (int j = 0; j < 2; ++j) {
            const int o = (lw * 2 + j) * 64 + lane;
            const int ln = idx * 472 + (o < 472 ? o : 0);
            l2_touch64(ln < 11008 ? (const char*)w2 + (size_t)ln * 128 : (const char*)wo + (size_t)(ln - 11008) * 128, lds0 + L_SCR);
        }
        wait_vm<10>(); kv_pub(4);
        wait_vm<2>(); kv_pub(5);
        __builtin_amdgcn_s_setprio(0);
    } else {
        const int tw = wid & 3, ns = wid >> 2;
        if (tid < 32) *(lu32*)((lds_ptr)lds + L_CTL + tid * 4) = 0u;
        tag = (unsigned)__builtin_amdgcn_readfirstlane(__hip_atomic_load((const gu32*)(ws + WS_EP), RLX_AGENT)) * 0x9E3779B1u + 0x7F4A7C15u;
        const int gtok = m0 + nt * 8 + (wid & 7);
        const u32x4 gx = *(const u32x4*)(x + (size_t)gtok * DM + lane * 8);
        const u32x4 gw = *(const u32x4*)(gate_w + lane * 8);
        float gs = 0.f;
#pragma unroll
        for (int q = 0; q < 4; ++q) { gs = fmaf(bflo(gx[q]), bflo(gw[q]), gs); gs = fmaf(bfhi(gx[q]), bfhi(gw[q]), gs); }
        gs = dpp_sum63(gs);
        PIN(gs);
        const int xrow = tw * 32 + r, xf7 = (xrow >> 1) & 7;
        int xa[4];
#pragma unroll
        for (int kk = 0; kk < 4; ++kk) xa[kk] = xrow * 128 + (((2 * kk + hi) ^ xf7) << 4);
        const int wa = G_XS + ((g4 >> 1) * 8 + (i16 >> 2)) * P_ROW + (ns * 32 + (g4 & 1) * 16 + (i16 & 3) * 4) * 2;
        f32x16 acc = f32x16{};
        GFrag fa, fb;
        LGKM_BAR();
        g_rd(fa, (lds_cptr)lds, xa, wa);
        TS(1);
        int so = G_STG;
#define GSTEP(FC, FN, ISV) { LGKM_BAR(); g_rd(FN, (lds_cptr)lds + so, xa, wa); so = so + G_STG == G_NSL * G_STG ? 0 : so + G_STG; g_mma<ISV>(acc, FC); }
        if (ns == 2) {
#pragma unroll 1
            for (int s = 0; s < 8; s += 2) { GSTEP(fa, fb, true) GSTEP(fb, fa, true) }
        } else {
#pragma unroll 1
            for (int s = 0; s < 8; s += 2) { GSTEP(fa, fb, false) GSTEP(fb, fa, false) }
        }
        TS(2);
        const int tt = mt * 4 + tw, h0 = nt * 2;
        if (ns < 2) {
            const float sc = ns == 0 ? QSCALE : 1.f;
            float ss[2];
#pragma unroll
            for (int e = 0; e < 2; ++e) {
                u32x4 p; float s2 = 0.f;
#pragma unroll
                for (int q = 0; q < 4; ++q) {
                    p[q] = cvtpk(acc[8 * e + 2 * q] * sc, acc[8 * e + 2 * q + 1] * sc);
                    const float a = bflo(p[q]), b = bfhi(p[q]);
                    s2 = fmaf(a, a, s2); s2 = fmaf(b, b, s2);
                }
                st16_sc1(ws + (ns == 0 ? WS_Q : WS_K) + (size_t)((h0 + e) * NTT + tt) * 1024, l16, p);
                ss[e] = s2;
            }
#pragma unroll
            for (int e = 0; e < 2; ++e) {
                ss[e] += xor32f(ss[e]);
                ss[e] = (ss[e] == ss[e]) ? ss[e] : __builtin_inff();
                ss[e] = dpp_max63(ss[e]);
            }
            if (lane == 63) {
                st4_sc1(ws + (ns == 0 ? WS_QST : WS_KST) + (size_t)(h0 * NTT + tt) * 4, ss[0]);
                st4_sc1(ws + (ns == 0 ? WS_QST : WS_KST) + (size_t)((h0 + 1) * NTT + tt) * 4, ss[1]);
            }
        } else {
            const unsigned vo = (unsigned)((r >> 4) * NTT * 1024 + hi * 256 + (r & 15) * 16);
            float vm = 0.f;
#pragma unroll
            for (int s2 = 0; s2 < 2; ++s2) {
                u32x4 pv;
#pragma unroll
                for (int q = 0; q < 4; ++q) {
                    const float a = acc[8 * s2 + 2 * q], b = acc[8 * s2 + 2 * q + 1];
                    pv[q] = cvtpk(a, b);
                    vm = fmaxf(vm, fmaxf((a == a) ? fabsf(a) : __builtin_inff(), (b == b) ? fabsf(b) : __builtin_inff()));
                }
                st16_sc1(ws + WS_V + (size_t)(h0 * NTT + tt) * 1024 + s2 * 512, vo, pv);
            }
            vm = dpp_max63(vm);
            if (lane == 63) st4_sc1(ws + WS_VST + (size_t)(nt * NTT + tt) * 4, vm);
        }
        if (wid < 8 && lane == 63) st4_sc1(ws + WS_G + (size_t)gtok * 4, 1.f / (1.f + __expf(-gs)));
        TS(3);
        acc = f32x16{};
        GSTEP(fa, fb, false)
        wait_vm<0>();
        GSTEP(fb, fa, false)
        raise_flag(ws + WS_FQ + nt * 256 + mt * 4, tag);
        GSTEP(fa, fb, false) GSTEP(fb, fa, false) GSTEP(fa, fb, false) GSTEP(fb, fa, false)
        u32x2 bia[2], bib[2];
        {
            int hi2 = tid; asm volatile("" : "+v"(hi2)); hi2 = (hi2 >> 5) & 1;
#pragma unroll
            for (int e = 0; e < 2; ++e) {
                int f = nt * P_BN + ns * 32 + 16 * e + 4 * hi2;
                f = f < FF ? f : 0;
                bia[e] = *(const u32x2*)(b1 + f);
                bib[e] = *(const u32x2*)(b1 + f + 8);
            }
        }
        GSTEP(fa, fb, false)
#undef GSTEP
        LGKM_BAR();
        if (wid == 0) {
#pragma unroll
            for (int j = 0; j < 2; ++j) {
                const unsigned one2 = ((lane & 15) == 0 || (lane & 15) == 4) ? 0x3f803f80u : 0u;
                *(__attribute__((address_space(3))) u32x4*)((lds_ptr)lds + A_LC + j * 1024 + lane * 16) = u32x4{one2, one2, one2, one2};
            }
            asm volatile("s_waitcnt lgkmcnt(0)" ::: "memory");
            if (lane == 0) *(volatile lu32*)((lds_ptr)lds + C_CST) = 1u;
        }
        g_mma<false>(acc, fb);
        TS(4);
#pragma unroll
        for (int e = 0; e < 2; ++e) {
            const int f0 = nt * P_BN + ns * 32 + 16 * e;
            const u32x2 ba = bia[e], bb = bib[e];
            u32x4 p;
            p[0] = cvtpk(mish_f(acc[8 * e + 0] + bflo(ba[0])), mish_f(acc[8 * e + 1] + bfhi(ba[0])));
            p[1] = cvtpk(mish_f(acc[8 * e + 2] + bflo(ba[1])), mish_f(acc[8 * e + 3] + bfhi(ba[1])));
            p[2] = cvtpk(mish_f(acc[8 * e + 4] + bflo(bb[0])), mish_f(acc[8 * e + 5] + bfhi(bb[0])));
            p[3] = cvtpk(mish_f(acc[8 * e + 6] + bflo(bb[1])), mish_f(acc[8 * e + 7] + bfhi(bb[1])));
            if (f0 < FF) st16_sc1(ws + WS_ACT + (size_t)(tt * FKS + (f0 >> 4)) * 1024, l16, p);
        }
    }
    auto act_done = [&]() {
        wait_vm<0>();
        if (lane == 0 && __hip_atomic_fetch_add((lu32*)((lds_ptr)lds + C_ACNT), 1u, RLX_WG) == 11u && nt < 15)
            __hip_atomic_store((gu32*)(ws + WS_FA + mt * 256 + nt * 4), tag, RLX_AGENT);
    };
    if (wid >= 4 && wid < 12) act_done();
    const int qp = wid & 3, kq = wid >> 2;
    {
        lds_wait_ge(lds, C_CST, 1u);
        lds_wait4_ge<2>(lds, C_KV, kq == 0 ? 1u : (unsigned)(kq + 2));
        TS(5);
        const bf16x8 qa = ldsfrag((lds_cptr)lds + A_LQ + qp * 2048 + lane * 16), qb = ldsfrag((lds_cptr)lds + A_LQ + qp * 2048 + 1024 + lane * 16);
        const bool fast = __builtin_amdgcn_readfirstlane(*(const lu32*)((lds_cptr)lds + C_FAST)) != 0;
        f32x16 oa = f32x16{}, ob = f32x16{};
        float ma = 0.f, mb = 0.f;
        const int kbase = kq * A_RG + lane * 16;
        const int vbase = r < 16 ? kq * A_RG + 1024 + hi * 256 + r * 16 : A_LC + hi * 256 + (r - 16) * 16;
        const int vinc = r < 16 ? 4096 : 0;
        const int voff1 = r < 16 ? 2048 : 1024;
        const f32x16 zero = f32x16{};
        if (fast) {
            lds_cptr kc = (lds_cptr)lds + kbase, vc = (lds_cptr)lds + vbase, vd = vc + voff1;
            bf16x8 kf = ldsfrag(kc);
            bf16x8 va0 = ldsfrag(vc), va1 = ldsfrag(vc + 512), vb0, vb1;
            f32x16 sa = MFMA(kf, qa, zero), sb = MFMA(kf, qb, zero);
            kf = ldsfrag(kc + 2048);
#define HALF(S, O, V0, V1)                                                              \
                _Pragma("unroll")                                                       \
                for (int i = 0; i < 8; ++i) S[i] = __builtin_amdgcn_exp2f(S[i]);        \
                O = MFMA(V0, pack8(S, 0), O);                                           \
                PIN(O); SBAR();                                                         \
                _Pragma("unroll")                                                       \
                for (int i = 8; i < 16; ++i) S[i] = __builtin_amdgcn_exp2f(S[i]);       \
                O = MFMA(V1, pack8(S, 1), O);                                           \
                PIN(O); SBAR();
#define TILE(VC0, VC1, VN0, VN1, KNEXT, VNEXT)                                          \
            {                                                                           \
                VN0 = ldsfrag(VNEXT); VN1 = ldsfrag((VNEXT) + 512);                     \
                SBAR();                                                                 \
                HALF(sa, oa, VC0, VC1)                                                  \
                sa = MFMA(kf, qa, zero); PIN(sa); SBAR();                               \
                HALF(sb, ob, VC0, VC1)                                                  \
                sb = MFMA(kf, qb, zero); PIN(sb);                                       \
                kf = ldsfrag(KNEXT);                                                    \
                SBAR();                                                                 \
            }
#pragma unroll 1
            for (int t = 0; t < 16; t += 2) {
                if (t == 2 && kq == 0) lds_wait4_ge(lds, C_KV, 2u);
                if (t == 6 && kq == 0) act_done();
                const int kn = t < 14 ? 4096 : 0;
                TILE(va0, va1, vb0, vb1, kc + kn, vd)
                vc += t < 14 ? vinc : 0;
                TILE(vb0, vb1, va0, va1, kc + kn + 2048, vc)
                kc += kn; vd += vinc;
            }
#undef TILE
#undef HALF
        } else {
            ma = -__builtin_inff(); mb = ma;
#pragma unroll 1
            for (int t = 0; t < 16; ++t) {
                if (t == 4 && kq == 0) { lds_wait4_ge(lds, C_KV, 2u); act_done(); }
                lds_cptr vc = (lds_cptr)lds + vbase + (r < 16 ? t * 2048 : 0);
                const bf16x8 kf = ldsfrag((lds_cptr)lds + kbase + t * 2048);
                const bf16x8 v0 = ldsfrag(vc), v1 = ldsfrag(vc + 512);
#define SLOW(Q, O, M)                                                                   \
                {                                                                       \
                    f32x16 s = MFMA(kf, Q, zero);                                       \
                    float mx = s[0];                                                    \
                    _Pragma("unroll")                                                   \
                    for (int i = 1; i < 16; ++i) mx = fmaxf(mx, s[i]);                  \
                    mx = fmaxf(mx, xor32f(mx));                                         \
                    const float mn = fmaxf(M, mx);                                      \
                    const float al = __builtin_amdgcn_exp2f(M - mn);                    \
                    M = mn;                                                             \
                    _Pragma("unroll")                                                   \
                    for (int i = 0; i < 16; ++i) { s[i] = __builtin_amdgcn_exp2f(s[i] - mn); O[i] *= al; } \
                    O = MFMA(v0, pack8(s, 0), O);                                       \
                    O = MFMA(v1, pack8(s, 1), O);                                       \
                }
                SLOW(qa, oa, ma)
                SLOW(qb, ob, mb)
#undef SLOW
            }
        }
        TS(6);
        {
            int tz = tid; asm volatile("" : "+v"(tz));
            lds_ptr ch = (lds_ptr)lds + A_CHAIN + qp * 5120 + (tz & 63) * 16;
            lds_ptr cl = (lds_ptr)lds + A_CHAIN + qp * 5120 + 4096 + (tz & 63) * 16;
            if (kq > 0) {
                lds_wait_ge(lds, C_CHAIN + qp * 4, (unsigned)kq);
                const f32x4 a0 = *(__attribute__((address_space(3))) f32x4*)(ch), a1 = *(__attribute__((address_space(3))) f32x4*)(ch + 1024);
                const f32x4 b0 = *(__attribute__((address_space(3))) f32x4*)(ch + 2048), b1v = *(__attribute__((address_space(3))) f32x4*)(ch + 3072);
                const f32x4 lm = *(__attribute__((address_space(3))) f32x4*)(cl);
                const float na = fmaxf(ma, lm[1]), nb = fmaxf(mb, lm[3]);
                const float fa = __builtin_amdgcn_exp2f(ma - na), xa2 = __builtin_amdgcn_exp2f(lm[1] - na);
                const float fb = __builtin_amdgcn_exp2f(mb - nb), xb2 = __builtin_amdgcn_exp2f(lm[3] - nb);
#pragma unroll
                for (int e = 0; e < 4; ++e) {
                    oa[e] = fmaf(a0[e], xa2, oa[e] * fa); oa[4 + e] = fmaf(a1[e], xa2, oa[4 + e] * fa);
                    ob[e] = fmaf(b0[e], xb2, ob[e] * fb); ob[4 + e] = fmaf(b1v[e], xb2, ob[4 + e] * fb);
                }
                oa[8] = fmaf(lm[0], xa2, oa[8] * fa); ob[8] = fmaf(lm[2], xb2, ob[8] * fb);
                ma = na; mb = nb;
            }
            if (kq < 3) {
                *(__attribute__((address_space(3))) f32x4*)(ch) = f32x4{oa[0], oa[1], oa[2], oa[3]};
                *(__attribute__((address_space(3))) f32x4*)(ch + 1024) = f32x4{oa[4], oa[5], oa[6], oa[7]};
                *(__attribute__((address_space(3))) f32x4*)(ch + 2048) = f32x4{ob[0], ob[1], ob[2], ob[3]};
                *(__attribute__((address_space(3))) f32x4*)(ch + 3072) = f32x4{ob[4], ob[5], ob[6], ob[7]};
                *(__attribute__((address_space(3))) f32x4*)(cl) = f32x4{oa[8], ma, ob[8], mb};
                lds_add1(lds, C_CHAIN + qp * 4, lane);
            } else {
                const float ra = 1.f / oa[8], rb = 1.f / ob[8];
                u32x4 pa, pb;
#pragma unroll
                for (int q = 0; q < 4; ++q) { pa[q] = cvtpk(oa[2 * q] * ra, oa[2 * q + 1] * ra); pb[q] = cvtpk(ob[2 * q] * rb, ob[2 * q + 1] * rb); }
                const int qt = qg * 8 + qp * 2;
                st16_sc1(ws + WS_O + (size_t)(qt * NH + head) * 1024, l16, pa);
                st16_sc1(ws + WS_O + (size_t)((qt + 1) * NH + head) * 1024, l16, pb);
            }
        }
        TS(7);
    }
    {
        int tz = tid; asm volatile("" : "+v"(tz));
        const unsigned lz16 = (tz & 63) * 16;
        const unsigned wl0 = (unsigned)((((tz >> 5) & 1) * 4 + ((tz >> 2) & 3)) * DM + nt4 * 64 + ((tz >> 4) & 1) * 32 + (tz & 3) * 8) * 2u;
        const char* aF0 = ws + WS_ACT + (size_t)(mt4 * 2 * FKS) * 1024;
        if (kq < 3) {
            if (wid == 0) {
                wait_flags(ws + WS_FA + (mt4 >> 1) * 256, 15, tag, lane);
                __builtin_amdgcn_fence(__ATOMIC_ACQUIRE, "agent");
                wait_vm<0>();
                if (lane == 0) *(volatile lu32*)((lds_ptr)lds + C_OACQ) = 1u;
#pragma unroll
                for (int j = 0; j < 3; ++j) {
                    const int o = j * 64 + lane;
                    l2_touch(aF0, (unsigned)(nt4 * 172 + (o < 172 ? o : 0)) * 128u, lds0 + L_SCR);
                }
            } else {
                lds_wait_ge(lds, C_OACQ, 1u);
            }
            lds_wait4_ge<4>(lds, C_CHAIN, (unsigned)(kq + 1));
#pragma unroll
            for (int j = 0; j < 4; ++j) {
                const int pc = qp + 4 * j;
                glds16(aF0 + (size_t)((pc >> 3) * FKS + (pc & 7) + kq * 8) * 1024, lz16, lds0 + kq * M_STG + pc * 1024);
                glds16(w2 + (size_t)(kq * 128 + pc * 8) * DM, wl0, lds0 + kq * M_STG + 16384 + pc * 1024);
            }
            wait_vm<0>();
        }
        const int wm = wid & 1, wn = (wid >> 1) & 1;
        const char* aF = aF0 + (size_t)((wid >> 3) * FKS + (wid & 7)) * 1024;
        const char* aO = ws + WS_O + (size_t)((mt4 * 2 + (wid >> 3)) * NH + (wid & 7)) * 1024;
        const u16* w2w = w2 + (size_t)wid * 8 * DM;
        const u16* wow = wo + (size_t)wid * 8 * DM;
        const unsigned lb = lds0 + wid * 1024;
        auto missue = [&](int s) {
            const int sa = (s == M_NS1 - 1 && (wid & 7) >= 6) ? s * 8 - 2 : s * 8;
            const int sw = (s == M_NS1 - 1 && wid >= 12) ? s * 128 - 32 : s * 128;
            glds16(aF + (size_t)sa * 1024, lz16, lb + (s & 3) * M_STG);
            glds16(w2w + (size_t)sw * DM, wl0, lb + (s & 3) * M_STG + 16384);
        };
        auto oissue_w = [&](int j) { glds16(wow + (size_t)j * 128 * DM, wl0, lb + ((M_NS1 + j) & 3) * M_STG + 16384); };
        auto oissue_a = [&](int j) { glds16c(aO + (size_t)j * 8192, lz16, lb + ((M_NS1 + j) & 3) * M_STG); };
        f32x16 acca = f32x16{}, accb = f32x16{};
        lds_cptr W = (lds_cptr)lds + 16384 + kq * 4096 + (((tz >> 5) & 1) * 2 + wn) * 256 + ((tz >> 2) & 3) * 64 + ((tz >> 4) & 1) * 32 + (tz & 3) * 8;
        lds_cptr A = (lds_cptr)lds + wm * 8192 + kq * 2048 + lz16;
        MFrag fa, fb;
        float g;
        LGKM_BAR();
        TS(8);
        missue(3);
        m_rd(fa, A, W);
#define MMA(FC, ACC) { SBAR(); ACC = MFMA(FC.w0, FC.a0, ACC); ACC = MFMA(FC.w1, FC.a1, ACC); PIN(ACC); SBAR(); }
#define RD(FN, S) m_rd(FN, A + ((S) & 3) * M_STG, W + ((S) & 3) * M_STG);
        LGKM_BAR(); missue(4); RD(fb, 1) MMA(fa, accb)
        LGKM_BAR(); missue(5); RD(fa, 2) MMA(fb, accb)
        wait_vm_lgkm_bar<4>(); missue(6); RD(fb, 3) MMA(fa, accb)
        raise_flag(ws + WS_FO + qg * 256 + head * 4, tag);
        wait_vm_lgkm_bar<4>(); missue(7); RD(fa, 4) MMA(fb, accb)
        wait_vm_lgkm_bar<4>(); missue(8); RD(fb, 5) MMA(fa, accb)
        wait_vm_lgkm_bar<4>(); missue(9); RD(fa, 6) MMA(fb, accb)
        wait_vm_lgkm_bar<4>(); missue(10); RD(fb, 7) MMA(fa, accb)
        wait_vm_lgkm_bar<4>();
        unsigned fv;
        asm volatile("global_load_dword %0, %1, %2 sc1" : "=v"(fv) : "v"((unsigned)(tz & 31) * 4u), "s"(ws + WS_FO + (mt4 >> 2) * 256) : "memory");
        oissue_w(0); RD(fa, 8) MMA(fb, accb)
        asm volatile("s_waitcnt vmcnt(1)" : "+v"(fv) :: "memory");
        if (wid == 0 && !__all(fv == tag)) wait_flags(ws + WS_FO + (mt4 >> 2) * 256, 32, tag, lane);
        TS(11);
        wait_vm_lgkm_bar<3>(); oissue_w(1); oissue_a(0); oissue_a(1); RD(fb, 9) MMA(fa, accb)
        wait_vm_lgkm_bar<4>(); oissue_w(2); oissue_a(2);
        asm volatile("global_load_dword %0, %1, %2 sc1" : "=v"(g) : "v"((unsigned)(mt4 * 64 + wm * 32 + (tz & 31)) * 4u), "s"(ws + WS_G) : "memory");
        RD(fa, 10) MMA(fb, accb)
        wait_vm_lgkm_bar<4>(); oissue_w(3); oissue_a(3); RD(fb, 11) if (kq < 3) MMA(fa, accb)
        wait_vm_lgkm_bar<5>(); RD(fa, 12) MMA(fb, acca)
        wait_vm_lgkm_bar<3>(); RD(fb, 13) MMA(fa, acca)
        asm volatile("s_waitcnt vmcnt(0) lgkmcnt(0)\n\ts_barrier" : "+v"(g) :: "memory");
        RD(fa, 14) MMA(fb, acca)
        MMA(fa, acca)
#undef RD
#undef MMA
        TS(9);
        const float g1 = 1.f - g;
        {
            lds_ptr red = (lds_ptr)lds + (wid & 3) * 3072 + lz16;
#pragma unroll
            for (int d = 0; d < 4; ++d) {
                if (d != kq) *(__attribute__((address_space(3))) f32x4*)(red + d * 12288 + (kq > d ? kq - 1 : kq) * 1024) =
                    f32x4{g * acca[4 * d] + g1 * accb[4 * d], g * acca[4 * d + 1] + g1 * accb[4 * d + 1], g * acca[4 * d + 2] + g1 * accb[4 * d + 2], g * acca[4 * d + 3] + g1 * accb[4 * d + 3]};
            }
        }
        f32x4 t;
#pragma unroll
        for (int e = 0; e < 4; ++e) {
            const float a = kq == 0 ? acca[e] : kq == 1 ? acca[4 + e] : kq == 2 ? acca[8 + e] : acca[12 + e];
            const float b = kq == 0 ? accb[e] : kq == 1 ? accb[4 + e] : kq == 2 ? accb[8 + e] : accb[12 + e];
            t[e] = g * a + g1 * b;
        }
        LGKM_BAR();
        lds_cptr src = (lds_cptr)lds + (wid & 3) * 3072 + kq * 12288 + lz16;
#pragma unroll
        for (int j = 0; j < 3; ++j) t += *(const __attribute__((address_space(3))) f32x4*)(src + j * 1024);
        u32x2 p; p[0] = cvtpk(t[0], t[1]); p[1] = cvtpk(t[2], t[3]);
        *(u32x2*)(out + (size_t)(mt4 * 64 + wm * 32 + (tz & 31)) * DM + nt4 * 64 + wn * 32 + ((tz >> 5) & 1) * 4 + kq * 8) = p;
    }
    if (blockIdx.x == 0 && tid == 0) __hip_atomic_store((gu32*)(ws + WS_EP), tag, RLX_AGENT);
    TS(10);
}

extern "C" void kernel_launch(void* const* d_in, const int* in_sizes, int n_in, void* d_out, int out_size, void* d_ws, size_t ws_size,
                              hipStream_t stream) {
    (void)hipFuncSetAttribute((const void*)fused_kernel, hipFuncAttributeMaxDynamicSharedMemorySize, LDS_BYTES);
    fused_kernel<<<256, 1024, LDS_BYTES, stream>>>((const u16*)d_in[0], (const u16*)d_in[1], (const u16*)d_in[2], (const u16*)d_in[4],
                                                   (const u16*)d_in[5], (const u16*)d_in[6], (const u16*)d_in[7], (char*)d_ws, (u16*)d_out);
}
```

```cpp
#include <hip/hip_runtime.h>
#include <hip/hip_bf16.h>
#include <stdint.h>

using bf16x8 = __attribute__((ext_vector_type(8))) short;
using s16x4  = __attribute__((ext_vector_type(4))) short;
using f32x16 = __attribute__((ext_vector_type(16))) float;
using f32x4  = __attribute__((ext_vector_type(4))) float;
using f32x2  = __attribute__((ext_vector_type(2))) float;
using u32x4  = __attribute__((ext_vector_type(4))) unsigned;
using u32x2  = __attribute__((ext_vector_type(2))) unsigned;
typedef __bf16 bf16x2_t __attribute__((ext_vector_type(2)));
typedef short v4i16_t __attribute__((ext_vector_type(4)));
typedef __attribute__((address_space(3))) const char* lds_cptr;
typedef __attribute__((address_space(3))) char* lds_ptr;
typedef unsigned short u16;

#define DI __device__ __forceinline__
#define TS(i)
#define TSEND(k)
#define MFMA(a, b, c) __builtin_amdgcn_mfma_f32_32x32x16_bf16((a), (b), (c), 0, 0, 0)
#define SBAR() __builtin_amdgcn_sched_barrier(0)
#define PIN(x) asm volatile("" : "+v"(x))

constexpr int SEQ = 2048, DM = 512, NH = 32, DH = 16, FF = 1376, NQKV = 1536;
constexpr int NTT = SEQ / 32;
constexpr int FKS = FF / 16;
constexpr float QSCALE = 0.25f * 1.4426950408889634f;

constexpr size_t WS_Q = 0;
constexpr size_t WS_K = WS_Q + (size_t)SEQ * DM * 2;
constexpr size_t WS_V = WS_K + (size_t)SEQ * DM * 2;
constexpr size_t WS_O = WS_V + (size_t)SEQ * DM * 2;
constexpr size_t WS_ACT = WS_O + (size_t)SEQ * DM * 2;
constexpr size_t WS_KST = WS_ACT + (size_t)SEQ * FF * 2;
constexpr size_t WS_VST = WS_KST + (size_t)NH * NTT * 4;
constexpr size_t WS_QST = WS_VST + (size_t)16 * NTT * 4;
constexpr size_t WS_G = WS_QST + (size_t)NH * NTT * 4;
constexpr size_t WS_FQ = WS_G + (size_t)SEQ * 4;
constexpr size_t WS_FA = WS_FQ + 16 * 256;
constexpr size_t WS_FO = WS_FA + 16 * 256;
constexpr size_t WS_EP = WS_FO + 8 * 256;
constexpr size_t WS_SCR = WS_EP + 256;

DI unsigned cvtpk(float lo, float hi) { f32x2 v = {lo, hi}; return __builtin_bit_cast(unsigned, __builtin_convertvector(v, bf16x2_t)); }
DI float bf2f(u16 v) { return __uint_as_float((unsigned)v << 16); }
DI float bflo(unsigned v) { return __uint_as_float(v << 16); }
DI float bfhi(unsigned v) { return __uint_as_float(v & 0xffff0000u); }
DI void glds16(const void* sbase, unsigned voff, unsigned lds_base) {
    unsigned sv;
    asm volatile("s_mov_b32 %0, m0\n\ts_mov_b32 m0, %3\n\ts_nop 0\n\tglobal_load_lds_dwordx4 %1, %2\n\ts_mov_b32 m0, %0"
                 : "=&s"(sv) : "v"(voff), "s"(sbase), "s"(lds_base) : "memory");
}
DI void glds16c(const void* sbase, unsigned voff, unsigned lds_base) {
    unsigned sv;
    asm volatile("s_mov_b32 %0, m0\n\ts_mov_b32 m0, %3\n\ts_nop 0\n\tglobal_load_lds_dwordx4 %1, %2 sc1\n\ts_mov_b32 m0, %0"
                 : "=&s"(sv) : "v"(voff), "s"(sbase), "s"(lds_base) : "memory");
}
template <int OFF> DI void gload16(bf16x8& dst, const void* sbase, unsigned voff) {
    asm volatile("global_load_dwordx4 %0, %1, %2 offset:%3" : "=v"(dst) : "v"(voff), "s"(sbase), "i"(OFF) : "memory");
}
DI s16x4 trread(lds_cptr p) { return __builtin_bit_cast(s16x4, __builtin_amdgcn_ds_read_tr16_b64_v4i16((__attribute__((address_space(3))) v4i16_t*)p)); }
DI bf16x8 ldsfrag(lds_cptr p) { return *(const __attribute__((address_space(3))) bf16x8*)p; }
DI bf16x8 cat8(s16x4 a, s16x4 b) { return __builtin_shufflevector(a, b, 0, 1, 2, 3, 4, 5, 6, 7); }
template <int N> DI void wait_vm_bar() { asm volatile("s_waitcnt vmcnt(%0)\n\ts_barrier" :: "i"(N) : "memory"); }
template <int N> DI void wait_vm_lgkm_bar() { asm volatile("s_waitcnt vmcnt(%0) lgkmcnt(0)\n\ts_barrier" :: "i"(N) : "memory"); }
DI float xor32f(float v) {
    auto r = __builtin_amdgcn_permlane32_swap(__float_as_uint(v), __float_as_uint(v), false, false);
    return (threadIdx.x & 32) ? __uint_as_float(r[0]) : __uint_as_float(r[1]);
}
DI float dpp_max63(float v) {
#define DSTEP(ctrl, rmask) v = fmaxf(v, __int_as_float(__builtin_amdgcn_update_dpp(__float_as_int(v), __float_as_int(v), ctrl, rmask, 0xf, false)))
    DSTEP(0x111, 0xf); DSTEP(0x112, 0xf); DSTEP(0x114, 0xf); DSTEP(0x118, 0xf); DSTEP(0x142, 0xa); DSTEP(0x143, 0xc);
#undef DSTEP
    return v;
}
DI float dpp_sum63(float v) {
#define DSTEP(ctrl, rmask) v += __int_as_float(__builtin_amdgcn_update_dpp(0, __float_as_int(v), ctrl, rmask, 0xf, false))
    DSTEP(0x111, 0xf); DSTEP(0x112, 0xf); DSTEP(0x114, 0xf); DSTEP(0x118, 0xf); DSTEP(0x142, 0xa); DSTEP(0x143, 0xc);
#undef DSTEP
    return v;
}
DI float bcast63(float v) { return __int_as_float(__builtin_amdgcn_readlane(__float_as_int(v), 63)); }
DI float mish_f(float h) {
    const float w = __builtin_amdgcn_exp2f(fminf(h * 1.4426950408889634f, 40.f));
    const float n = fmaf(w, w, w + w);
    return h * (n * __builtin_amdgcn_rcpf(n + 2.f));
}

typedef __attribute__((address_space(1))) unsigned gu32;
#define RLX_AGENT __ATOMIC_RELAXED, __HIP_MEMORY_SCOPE_AGENT
DI void st16_sc1(void* sbase, unsigned voff, u32x4 v) {
    asm volatile("global_store_dwordx4 %0, %1, %2 sc1\n\ts_nop 1" :: "v"(voff), "v"(v), "s"(sbase) : "memory");
}
DI void st4_sc1(void* p, float v) { __hip_atomic_store((gu32*)p, __float_as_uint(v), RLX_AGENT); }
DI void publish(void* flag, unsigned tag) {
    asm volatile("s_waitcnt vmcnt(0) lgkmcnt(0)\n\ts_barrier" ::: "memory");
    if (threadIdx.x == 0) __hip_atomic_store((gu32*)flag, tag, RLX_AGENT);
}
DI void wait_flags(const void* flags, int n, unsigned tag, int lane) {
    for (unsigned spins = 0; spins < (1u << 18); ++spins) {
        unsigned v = tag;
        if (lane < n) v = __hip_atomic_load((const gu32*)flags + lane, RLX_AGENT);
        if (__all(v == tag)) break;
        __builtin_amdgcn_s_sleep(2);
    }
    __builtin_amdgcn_fence(__ATOMIC_ACQUIRE, "wavefront");
    asm volatile("" ::: "memory");
}

constexpr int P_BN = 96, P_ROW = P_BN * 2;
constexpr int G_XS = 16384, G_STG = G_XS + 64 * P_ROW, G_NSL = 5, LDS_BYTES = 163840;
constexpr int A_RG = 32768, A_LC = 4 * A_RG, A_CHAIN = A_LC + 2048;
constexpr int A_LQ = A_CHAIN + 20480, L_CTL = A_LQ + 8192;
constexpr int C_FAST = L_CTL, C_OACQ = L_CTL + 4, C_OCNT = L_CTL + 8, C_WOR = L_CTL + 12, C_CHAIN = L_CTL + 16, C_FULL = L_CTL + 32, C_FREE = L_CTL + 48, C_KV = L_CTL + 64, C_G2 = L_CTL + 80, C_QACQ = L_CTL + 84, C_ACNT = L_CTL + 88, C_CST = L_CTL + 92, C_XCNT = L_CTL + 96;
constexpr int M_STG = 32768, M_NS1 = (FF + 127) / 128, M_NS = M_NS1 + DM / 128;
constexpr float A_THR = 40.f;
static_assert(L_CTL >= G_NSL * G_STG && L_CTL + 128 <= LDS_BYTES - 256, "lds map");
typedef __attribute__((address_space(3))) unsigned lu32;
#define RLX_WG __ATOMIC_RELAXED, __HIP_MEMORY_SCOPE_WORKGROUP

DI bf16x8 pack8(const f32x16& p, int s) {
    u32x4 r;
#pragma unroll
    for (int q = 0; q < 4; ++q) r[q] = cvtpk(p[8 * s + 2 * q], p[8 * s + 2 * q + 1]);
    return __builtin_bit_cast(bf16x8, r);
}
#define LGKM_BAR() asm volatile("s_waitcnt lgkmcnt(0)\n\ts_barrier" ::: "memory")
#define BAR() asm volatile("s_barrier" ::: "memory")
template <int N> DI void wait_vm() { asm volatile("s_waitcnt vmcnt(%0)" :: "i"(N) : "memory"); }
DI void raise_flag(void* flag, unsigned tag) { if (threadIdx.x == 0) __hip_atomic_store((gu32*)flag, tag, RLX_AGENT); }
DI void lds_wait_ge(char* lds, int off, unsigned want) {
    asm volatile("" ::: "memory");
    while ((unsigned)__builtin_amdgcn_readfirstlane(__hip_atomic_load((lu32*)((lds_ptr)lds + off), RLX_WG)) < want) __builtin_amdgcn_s_sleep(4);
    asm volatile("" ::: "memory");
}
template <int SLEEP = 1>
DI void lds_wait4_ge(char* lds, int off, unsigned want) {
    asm volatile("" ::: "memory");
    for (;;) {
        const u32x4 v = *(volatile __attribute__((address_space(3))) u32x4*)((lds_ptr)lds + off);
        const unsigned m = min(min(v[0], v[1]), min(v[2], v[3]));
        if ((unsigned)__builtin_amdgcn_readfirstlane(m) >= want) break;
        __builtin_amdgcn_s_sleep(SLEEP);
    }
    asm volatile("" ::: "memory");
}
DI void lds_add1(char* lds, int off, int lane) {
    asm volatile("s_waitcnt lgkmcnt(0)" ::: "memory");
    if (lane == 0) __hip_atomic_fetch_add((lu32*)((lds_ptr)lds + off), 1u, RLX_WG);
}
DI bool try_flags(const void* flags, int n, unsigned tag, int lane) {
    unsigned v = tag;
    if (lane < n) v = __hip_atomic_load((const gu32*)flags + lane, RLX_AGENT);
    if (!__all(v == tag)) return false;
    __builtin_amdgcn_fence(__ATOMIC_ACQUIRE, "agent");
    return true;
}

DI void l2_touch(const void* sbase, unsigned voff, unsigned lds_scratch) {
    unsigned sv;
    asm volatile("s_mov_b32 %0, m0\n\ts_mov_b32 m0, %3\n\ts_nop 0\n\tglobal_load_lds_dword %1, %2\n\ts_mov_b32 m0, %0"
                 : "=&s"(sv) : "v"(voff), "s"(sbase), "s"(lds_scratch) : "memory");
}
DI void l2_touch64(const void* p, unsigned lds_scratch) {
    unsigned sv;
    asm volatile("s_mov_b32 %0, m0\n\ts_mov_b32 m0, %2\n\ts_nop 0\n\tglobal_load_lds_dword %1, off\n\ts_mov_b32 m0, %0"
                 : "=&s"(sv) : "v"(p), "s"(lds_scratch) : "memory");
}
constexpr int L_SCR = 163840 - 256;
struct GFrag { bf16x8 x[4]; bf16x8 w[4]; };
DI void g_rd(GFrag& f, lds_cptr b, const int (&xa)[4], int wa) {
#pragma unroll
    for (int kk = 0; kk < 4; ++kk) {
        f.x[kk] = ldsfrag(b + xa[kk]);
        f.w[kk] = cat8(trread(b + wa + kk * 16 * P_ROW), trread(b + wa + (kk * 16 + 4) * P_ROW));
    }
}
template <bool ISV> DI void g_mma(f32x16& acc, const GFrag& f) {
    SBAR();
#pragma unroll
    for (int kk = 0; kk < 4; ++kk) { if (ISV) acc = MFMA(f.x[kk], f.w[kk], acc); else acc = MFMA(f.w[kk], f.x[kk], acc); }
    PIN(acc);
    SBAR();
}
struct MFrag { bf16x8 a0, a1, w0, w1; };
DI void m_rd(MFrag& f, lds_cptr A, lds_cptr W) {
    f.w0 = cat8(trread(W), trread(W + 1024)); f.a0 = ldsfrag(A);
    f.w1 = cat8(trread(W + 2048), trread(W + 3072)); f.a1 = ldsfrag(A + 1024);
}

__global__ __launch_bounds__(1024, 4) void fused_kernel(const u16* __restrict__ x, const u16* __restrict__ wqkv, const u16* __restrict__ wo,
                                                        const u16* __restrict__ w1, const u16* __restrict__ b1, const u16* __restrict__ w2,
                                                        const u16* __restrict__ gate_w, char* __restrict__ ws, u16* __restrict__ out) {
    extern __shared__ __attribute__((aligned(16))) char lds[];
    const int tid = threadIdx.x, lane = tid & 63, wid = __builtin_amdgcn_readfirstlane(tid >> 6);
    const int r = lane & 31, hi = lane >> 5;
    const int xg = blockIdx.x & 7, idx = blockIdx.x >> 3;
    const unsigned lds0 = (unsigned)(uintptr_t)lds;
    const unsigned l16 = lane * 16;
    const int i16 = lane & 15, g4 = lane >> 4;
    TS(0);
    const int mt = (xg >> 1) * 4 + (idx & 3), nt = (xg & 1) * 8 + (idx >> 2);
    const int head = xg * 4 + (idx & 3), qg = idx >> 2;
    const int mt4 = xg * 4 + (idx & 3), nt4 = idx >> 2;
    const int m0 = mt * 128;
    unsigned tag;

    if (wid >= 12) {
        const int lw = wid - 12;
        __builtin_amdgcn_s_setprio(3);
        const int rot = ((idx >> 2) + 2 * (idx & 3)) & 7;
        unsigned epv;
        asm volatile("global_load_dword %0, %1, %2 sc1" : "=v"(epv) : "v"(0u), "s"(ws + WS_EP) : "memory");
        unsigned woq[3], wof[3];
#pragma unroll
        for (int j = 0; j < 3; ++j) {
            const int o = (lw * 3 + j) * 1024 + lane * 16;
            const int k = o / P_ROW, cc = (o % P_ROW) >> 4;
            woq[j] = (unsigned)(k * NQKV + (cc >> 2) * 512 + nt * 32 + (cc & 3) * 8) * 2u;
            int n = nt * P_BN + cc * 8;
            n = n > FF - 8 ? FF - 8 : n;
            wof[j] = (unsigned)(k * FF + n) * 2u;
        }
        const unsigned xoff = (unsigned)((m0 + lw * 32 + (lane >> 3)) * DM) * 2u + (((lane & 7) ^ g4) << 4), xoff1 = xoff ^ 64u;
        auto issue = [&](int s, int slot) {
            const int kb = (s + rot) & 7;
            const unsigned base = lds0 + slot * G_STG;
            const u16* xs = x + kb * 64;
            glds16(xs, xoff, base + lw * 4096);
            glds16(xs + 8 * DM, xoff1, base + lw * 4096 + 1024);
            glds16(xs + 16 * DM, xoff, base + lw * 4096 + 2048);
            glds16(xs + 24 * DM, xoff1, base + lw * 4096 + 3072);
            const u16* wsrc = s < 8 ? wqkv + (size_t)kb * 64 * NQKV : w1 + (size_t)kb * 64 * FF;
#pragma unroll
            for (int j = 0; j < 3; ++j) glds16(wsrc, s < 8 ? woq[j] : wof[j], base + G_XS + (lw * 3 + j) * 1024);
        };
        issue(0, 0); issue(1, 1);
        asm volatile("s_waitcnt vmcnt(7)\n\ts_barrier" : "+v"(epv) :: "memory");
        tag = (unsigned)__builtin_amdgcn_readfirstlane(epv) * 0x9E3779B1u + 0x7F4A7C15u;
        issue(2, 2); issue(3, 3);
        wait_vm<14>(); BAR();
        issue(4, 4); issue(5, 0);
        {
            const int ln = idx * 192 + (lw < 3 ? lw * 64 + lane : 0), row = ln / 12;
            int col = (xg & 1) * 768 + (ln - row * 12) * 64;
            col = col > FF - 2 ? FF - 2 : col;
            l2_touch(w1, (unsigned)(row * FF + col) * 2u, lds0 + L_SCR);
        }
        wait_vm<22>(); BAR(); issue(6, 1);
        wait_vm<22>(); BAR(); issue(7, 2);
        wait_vm<22>(); BAR(); issue(8, 3);
        wait_vm<22>(); BAR(); issue(9, 4);
        int slot = 0;
#pragma unroll 1
        for (int s = 5; s < 11; ++s) {
            wait_vm<21>(); BAR();
            issue(s + G_NSL, slot);
            slot = slot + 1 == G_NSL ? 0 : slot + 1;
        }
        wait_vm<21>(); BAR();
        wait_vm<14>(); BAR();
        const void* fq = ws + WS_FQ + (head >> 1) * 256;
        bool seen = false;
        if (lw == 3) { unsigned v = tag; if (lane < 16) v = __hip_atomic_load((const gu32*)fq + lane, RLX_AGENT); seen = __all(v == tag); }
        wait_vm<7>(); BAR();
        if (lw == 3 && !seen) wait_flags(fq, 16, tag, lane);
        wait_vm<0>(); BAR();
        float kst, vst, qst;
        asm volatile("global_load_dword %0, %1, %2 sc1" : "=v"(kst) : "v"((unsigned)lane * 4u), "s"(ws + WS_KST + head * 256) : "memory");
        asm volatile("global_load_dword %0, %1, %2 sc1" : "=v"(vst) : "v"((unsigned)lane * 4u), "s"(ws + WS_VST + (head >> 1) * 256) : "memory");
        asm volatile("global_load_dword %0, %1, %2 sc1" : "=v"(qst) : "v"((unsigned)(lane & 7) * 4u), "s"(ws + WS_QST + head * 256 + qg * 32) : "memory");
        glds16c(ws + WS_Q + (size_t)(head * NTT + qg * 8 + lw * 2) * 1024, l16, lds0 + A_LQ + lw * 2048);
        glds16c(ws + WS_Q + (size_t)(head * NTT + qg * 8 + lw * 2 + 1) * 1024, l16, lds0 + A_LQ + lw * 2048 + 1024);
        const char* ksrc = ws + WS_K + (size_t)(head * NTT + lw) * 1024;
        const char* vsrc = ws + WS_V + (size_t)(head * NTT + lw) * 1024;
        auto kv = [&](int t) {
            const unsigned dst = lds0 + (((t >> 4) + 1) & 3) * A_RG + ((t & 15) + lw) * 2048;
            glds16c(ksrc + t * 1024, l16, dst);
            glds16c(vsrc + t * 1024, l16, dst + 1024);
        };
        auto kv_pub = [&](unsigned n) { if (lane == 0) *(volatile lu32*)((lds_ptr)lds + C_KV + lw * 4) = n; };
        kv(0); kv(16); kv(32);
        asm volatile("s_waitcnt vmcnt(8)" : "+v"(kst), "+v"(vst), "+v"(qst) :: "memory");
        if (lw == 0) {
            const float kmax = dpp_max63(kst), vmax = dpp_max63(vst), qmax = dpp_max63(qst);
            if (lane == 63) *(volatile lu32*)((lds_ptr)lds + C_FAST) = (qmax * kmax * 1.01f <= A_THR * A_THR && vmax <= 1e18f) ? 1u : 0u;
            asm volatile("s_waitcnt lgkmcnt(0)" ::: "memory");
        }
        lds_wait_ge(lds, C_XCNT, 12u);
        __builtin_amdgcn_s_setprio(0);
        kv(48);
        wait_vm<2>(); kv_pub(1);
        kv(4); kv(20); kv(36); kv(52);
        wait_vm<8>(); kv_pub(2);
        kv(8); kv(24); kv(40); kv(56);
        wait_vm<8>(); kv_pub(3);
        kv(12); kv(28); kv(44); kv(60);
#pragma unroll
        for (int j = 0; j < 2; ++j) {
            const int o = (lw * 2 + j) * 64 + lane;
            const int ln = idx * 472 + (o < 472 ? o : 0);
            l2_touch64(ln < 11008 ? (const char*)w2 + (size_t)ln * 128 : (const char*)wo + (size_t)(ln - 11008) * 128, lds0 + L_SCR);
        }
        wait_vm<10>(); kv_pub(4);
        wait_vm<2>(); kv_pub(5);
        __builtin_amdgcn_s_setprio(0);
    } else {
        const int tw = wid & 3, ns = wid >> 2;
        if (tid < 32) *(lu32*)((lds_ptr)lds + L_CTL + tid * 4) = 0u;
        tag = (unsigned)__builtin_amdgcn_readfirstlane(__hip_atomic_load((const gu32*)(ws + WS_EP), RLX_AGENT)) * 0x9E3779B1u + 0x7F4A7C15u;
        const int gtok = m0 + nt * 8 + (wid & 7);
        const u32x4 gx = *(const u32x4*)(x + (size_t)gtok * DM + lane * 8);
        const u32x4 gw = *(const u32x4*)(gate_w + lane * 8);
        float gs = 0.f;
#pragma unroll
        for (int q = 0; q < 4; ++q) { gs = fmaf(bflo(gx[q]), bflo(gw[q]), gs); gs = fmaf(bfhi(gx[q]), bfhi(gw[q]), gs); }
        gs = dpp_sum63(gs);
        PIN(gs);
        const int xrow = tw * 32 + r, xf7 = (xrow >> 1) & 7;
        int xa[4];
#pragma unroll
        for (int kk = 0; kk < 4; ++kk) xa[kk] = xrow * 128 + (((2 * kk + hi) ^ xf7) << 4);
        const int wa = G_XS + ((g4 >> 1) * 8 + (i16 >> 2)) * P_ROW + (ns * 32 + (g4 & 1) * 16 + (i16 & 3) * 4) * 2;
        f32x16 acc = f32x16{};
        GFrag fa, fb;
        LGKM_BAR();
        g_rd(fa, (lds_cptr)lds, xa, wa);
        TS(1);
        int so = G_STG;
#define GSTEP(FC, FN, ISV) { LGKM_BAR(); g_rd(FN, (lds_cptr)lds + so, xa, wa); so = so + G_STG == G_NSL * G_STG ? 0 : so + G_STG; g_mma<ISV>(acc, FC); }
        if (ns == 2) {
#pragma unroll 1
            for (int s = 0; s < 8; s += 2) { GSTEP(fa, fb, true) GSTEP(fb, fa, true) }
        } else {
#pragma unroll 1
            for (int s = 0; s < 8; s += 2) { GSTEP(fa, fb, false) GSTEP(fb, fa, false) }
        }
        TS(2);
        const int tt = mt * 4 + tw, h0 = nt * 2;
        if (ns < 2) {
            const float sc = ns == 0 ? QSCALE : 1.f;
            float ss[2];
#pragma unroll
            for (int e = 0; e < 2; ++e) {
                u32x4 p; float s2 = 0.f;
#pragma unroll
                for (int q = 0; q < 4; ++q) {
                    p[q] = cvtpk(acc[8 * e + 2 * q] * sc, acc[8 * e + 2 * q + 1] * sc);
                    const float a = bflo(p[q]), b = bfhi(p[q]);
                    s2 = fmaf(a, a, s2); s2 = fmaf(b, b, s2);
                }
                st16_sc1(ws + (ns == 0 ? WS_Q : WS_K) + (size_t)((h0 + e) * NTT + tt) * 1024, l16, p);
                ss[e] = s2;
            }
#pragma unroll
            for (int e = 0; e < 2; ++e) {
                ss[e] += xor32f(ss[e]);
                ss[e] = (ss[e] == ss[e]) ? ss[e] : __builtin_inff();
                ss[e] = dpp_max63(ss[e]);
            }
            if (lane == 63) {
                st4_sc1(ws + (ns == 0 ? WS_QST : WS_KST) + (size_t)(h0 * NTT + tt) * 4, ss[0]);
                st4_sc1(ws + (ns == 0 ? WS_QST : WS_KST) + (size_t)((h0 + 1) * NTT + tt) * 4, ss[1]);
            }
        } else {
            const unsigned vo = (unsigned)((r >> 4) * NTT * 1024 + hi * 256 + (r & 15) * 16);
            float vm = 0.f;
#pragma unroll
            for (int s2 = 0; s2 < 2; ++s2) {
                u32x4 pv;
#pragma unroll
                for (int q = 0; q < 4; ++q) {
                    const float a = acc[8 * s2 + 2 * q], b = acc[8 * s2 + 2 * q + 1];
                    pv[q] = cvtpk(a, b);
                    vm = fmaxf(vm, fmaxf((a == a) ? fabsf(a) : __builtin_inff(), (b == b) ? fabsf(b) : __builtin_inff()));
                }
                st16_sc1(ws + WS_V + (size_t)(h0 * NTT + tt) * 1024 + s2 * 512, vo, pv);
            }
            vm = dpp_max63(vm);
            if (lane == 63) st4_sc1(ws + WS_VST + (size_t)(nt * NTT + tt) * 4, vm);
        }
        if (wid < 8 && lane == 63) st4_sc1(ws + WS_G + (size_t)gtok * 4, 1.f / (1.f + __expf(-gs)));
        TS(3);
        acc = f32x16{};
        GSTEP(fa, fb, false)
        wait_vm<0>();
        GSTEP(fb, fa, false)
        raise_flag(ws + WS_FQ + nt * 256 + mt * 4, tag);
        GSTEP(fa, fb, false) GSTEP(fb, fa, false) GSTEP(fa, fb, false) GSTEP(fb, fa, false)
        u32x2 bia[2], bib[2];
        {
            int hi2 = tid; asm volatile("" : "+v"(hi2)); hi2 = (hi2 >> 5) & 1;
#pragma unroll
            for (int e = 0; e < 2; ++e) {
                int f = nt * P_BN + ns * 32 + 16 * e + 4 * hi2;
                f = f < FF ? f : 0;
                bia[e] = *(const u32x2*)(b1 + f);
                bib[e] = *(const u32x2*)(b1 + f + 8);
            }
        }
        GSTEP(fa, fb, false)
#undef GSTEP
        lds_add1(lds, C_XCNT, lane);
        if (wid == 0) {
#pragma unroll
            for (int j = 0; j < 2; ++j) {
                const unsigned one2 = ((lane & 15) == 0 || (lane & 15) == 4) ? 0x3f803f80u : 0u;
                *(__attribute__((address_space(3))) u32x4*)((lds_ptr)lds + A_LC + j * 1024 + lane * 16) = u32x4{one2, one2, one2, one2};
            }
            asm volatile("s_waitcnt lgkmcnt(0)" ::: "memory");
            if (lane == 0) *(volatile lu32*)((lds_ptr)lds + C_CST) = 1u;
        }
        g_mma<false>(acc, fb);
        TS(4);
#pragma unroll
        for (int e = 0; e < 2; ++e) {
            const int f0 = nt * P_BN + ns * 32 + 16 * e;
            const u32x2 ba = bia[e], bb = bib[e];
            u32x4 p;
            p[0] = cvtpk(mish_f(acc[8 * e + 0] + bflo(ba[0])), mish_f(acc[8 * e + 1] + bfhi(ba[0])));
            p[1] = cvtpk(mish_f(acc[8 * e + 2] + bflo(ba[1])), mish_f(acc[8 * e + 3] + bfhi(ba[1])));
            p[2] = cvtpk(mish_f(acc[8 * e + 4] + bflo(bb[0])), mish_f(acc[8 * e + 5] + bfhi(bb[0])));
            p[3] = cvtpk(mish_f(acc[8 * e + 6] + bflo(bb[1])), mish_f(acc[8 * e + 7] + bfhi(bb[1])));
            if (f0 < FF) st16_sc1(ws + WS_ACT + (size_t)(tt * FKS + (f0 >> 4)) * 1024, l16, p);
        }
    }
    auto act_done = [&]() {
        wait_vm<0>();
        if (lane == 0 && __hip_atomic_fetch_add((lu32*)((lds_ptr)lds + C_ACNT), 1u, RLX_WG) == 11u && nt < 15)
            __hip_atomic_store((gu32*)(ws + WS_FA + mt * 256 + nt * 4), tag, RLX_AGENT);
    };
    const int qp = wid & 3, kq = wid >> 2;
    {
        lds_wait_ge(lds, C_CST, 1u);
        lds_wait4_ge<2>(lds, C_KV, kq == 3 ? 2u : 1u);
        TS(5);
        const bf16x8 qa = ldsfrag((lds_cptr)lds + A_LQ + qp * 2048 + lane * 16), qb = ldsfrag((lds_cptr)lds + A_LQ + qp * 2048 + 1024 + lane * 16);
        const bool fast = __builtin_amdgcn_readfirstlane(*(const lu32*)((lds_cptr)lds + C_FAST)) != 0;
        f32x16 oa = f32x16{}, ob = f32x16{};
        float ma = 0.f, mb = 0.f;
        const int rg = (kq + 1) & 3;
        const int kbase = rg * A_RG + lane * 16;
        const int vbase = r < 16 ? rg * A_RG + 1024 + hi * 256 + r * 16 : A_LC + hi * 256 + (r - 16) * 16;
        const int vinc = r < 16 ? 4096 : 0;
        const int voff1 = r < 16 ? 2048 : 1024;
        const f32x16 zero = f32x16{};
        if (fast) {
            lds_cptr kc = (lds_cptr)lds + kbase, vc = (lds_cptr)lds + vbase, vd = vc + voff1;
            bf16x8 kf = ldsfrag(kc);
            bf16x8 va0 = ldsfrag(vc), va1 = ldsfrag(vc + 512), vb0, vb1;
            f32x16 sa = MFMA(kf, qa, zero), sb = MFMA(kf, qb, zero);
            kf = ldsfrag(kc + 2048);
#define HALF(S, O, V0, V1)                                                              \
                _Pragma("unroll")                                                       \
                for (int i = 0; i < 8; ++i) S[i] = __builtin_amdgcn_exp2f(S[i]);        \
                O = MFMA(V0, pack8(S, 0), O);                                           \
                PIN(O); SBAR();                                                         \
                _Pragma("unroll")                                                       \
                for (int i = 8; i < 16; ++i) S[i] = __builtin_amdgcn_exp2f(S[i]);       \
                O = MFMA(V1, pack8(S, 1), O);                                           \
                PIN(O); SBAR();
#define TILE(VC0, VC1, VN0, VN1, KNEXT, VNEXT)                                          \
            {                                                                           \
                VN0 = ldsfrag(VNEXT); VN1 = ldsfrag((VNEXT) + 512);                     \
                SBAR();                                                                 \
                HALF(sa, oa, VC0, VC1)                                                  \
                sa = MFMA(kf, qa, zero); PIN(sa); SBAR();                               \
                HALF(sb, ob, VC0, VC1)                                                  \
                sb = MFMA(kf, qb, zero); PIN(sb);                                       \
                kf = ldsfrag(KNEXT);                                                    \
                SBAR();                                                                 \
            }
#pragma unroll 1
            for (int t = 0; t < 16; t += 2) {
                if (t == 0) __builtin_amdgcn_s_setprio(3); else if (t == 4) __builtin_amdgcn_s_setprio(2); else if (t == 8) __builtin_amdgcn_s_setprio(1); else if (t == 12) __builtin_amdgcn_s_setprio(0);
                if ((t & 3) == 2 && t < 14) lds_wait4_ge(lds, C_KV, (unsigned)((t >> 2) + 3));
                if (t == 6 && kq < 3) act_done();
                const int kn = t < 14 ? 4096 : 0;
                TILE(va0, va1, vb0, vb1, kc + kn, vd)
                vc += t < 14 ? vinc : 0;
                TILE(vb0, vb1, va0, va1, kc + kn + 2048, vc)
                kc += kn; vd += vinc;
            }
#undef TILE
#undef HALF
        } else {
            ma = -__builtin_inff(); mb = ma;
#pragma unroll 1
            for (int t = 0; t < 16; ++t) {
                if ((t & 3) == 0 && t > 0) lds_wait4_ge(lds, C_KV, (unsigned)((t >> 2) + 2));
                if (t == 6 && kq < 3) act_done();
                lds_cptr vc = (lds_cptr)lds + vbase + (r < 16 ? t * 2048 : 0);
                const bf16x8 kf = ldsfrag((lds_cptr)lds + kbase + t * 2048);
                const bf16x8 v0 = ldsfrag(vc), v1 = ldsfrag(vc + 512);
#define SLOW(Q, O, M)                                                                   \
                {                                                                       \
                    f32x16 s = MFMA(kf, Q, zero);                                       \
                    float mx = s[0];                                                    \
                    _Pragma("unroll")                                                   \
                    for (int i = 1; i < 16; ++i) mx = fmaxf(mx, s[i]);                  \
                    mx = fmaxf(mx, xor32f(mx));                                         \
                    const float mn = fmaxf(M, mx);                                      \
                    const float al = __builtin_amdgcn_exp2f(M - mn);                    \
                    M = mn;                                                             \
                    _Pragma("unroll")                                                   \
                    for (int i = 0; i < 16; ++i) { s[i] = __builtin_amdgcn_exp2f(s[i] - mn); O[i] *= al; } \
                    O = MFMA(v0, pack8(s, 0), O);                                       \
                    O = MFMA(v1, pack8(s, 1), O);                                       \
                }
                SLOW(qa, oa, ma)
                SLOW(qb, ob, mb)
#undef SLOW
            }
        }
        TS(6);
        {
            int tz = tid; asm volatile("" : "+v"(tz));
            lds_ptr ch = (lds_ptr)lds + A_CHAIN + qp * 5120 + (tz & 63) * 16;
            lds_ptr cl = (lds_ptr)lds + A_CHAIN + qp * 5120 + 4096 + (tz & 63) * 16;
            if (kq > 0) {
                lds_wait_ge(lds, C_CHAIN + qp * 4, (unsigned)kq);
                const f32x4 a0 = *(__attribute__((address_space(3))) f32x4*)(ch), a1 = *(__attribute__((address_space(3))) f32x4*)(ch + 1024);
                const f32x4 b0 = *(__attribute__((address_space(3))) f32x4*)(ch + 2048), b1v = *(__attribute__((address_space(3))) f32x4*)(ch + 3072);
                const f32x4 lm = *(__attribute__((address_space(3))) f32x4*)(cl);
                const float na = fmaxf(ma, lm[1]), nb = fmaxf(mb, lm[3]);
                const float fa = __builtin_amdgcn_exp2f(ma - na), xa2 = __builtin_amdgcn_exp2f(lm[1] - na);
                const float fb = __builtin_amdgcn_exp2f(mb - nb), xb2 = __builtin_amdgcn_exp2f(lm[3] - nb);
#pragma unroll
                for (int e = 0; e < 4; ++e) {
                    oa[e] = fmaf(a0[e], xa2, oa[e] * fa); oa[4 + e] = fmaf(a1[e], xa2, oa[4 + e] * fa);
                    ob[e] = fmaf(b0[e], xb2, ob[e] * fb); ob[4 + e] = fmaf(b1v[e], xb2, ob[4 + e] * fb);
                }
                oa[8] = fmaf(lm[0], xa2, oa[8] * fa); ob[8] = fmaf(lm[2], xb2, ob[8] * fb);
                ma = na; mb = nb;
            }
            if (kq < 3) {
                *(__attribute__((address_space(3))) f32x4*)(ch) = f32x4{oa[0], oa[1], oa[2], oa[3]};
                *(__attribute__((address_space(3))) f32x4*)(ch + 1024) = f32x4{oa[4], oa[5], oa[6], oa[7]};
                *(__attribute__((address_space(3))) f32x4*)(ch + 2048) = f32x4{ob[0], ob[1], ob[2], ob[3]};
                *(__attribute__((address_space(3))) f32x4*)(ch + 3072) = f32x4{ob[4], ob[5], ob[6], ob[7]};
                *(__attribute__((address_space(3))) f32x4*)(cl) = f32x4{oa[8], ma, ob[8], mb};
                lds_add1(lds, C_CHAIN + qp * 4, lane);
            } else {
                const float ra = 1.f / oa[8], rb = 1.f / ob[8];
                u32x4 pa, pb;
#pragma unroll
                for (int q = 0; q < 4; ++q) { pa[q] = cvtpk(oa[2 * q] * ra, oa[2 * q + 1] * ra); pb[q] = cvtpk(ob[2 * q] * rb, ob[2 * q + 1] * rb); }
                const int qt = qg * 8 + qp * 2;
                st16_sc1(ws + WS_O + (size_t)(qt * NH + head) * 1024, l16, pa);
                st16_sc1(ws + WS_O + (size_t)((qt + 1) * NH + head) * 1024, l16, pb);
            }
        }
        TS(7);
    }
    {
        int tz = tid; asm volatile("" : "+v"(tz));
        const unsigned lz16 = (tz & 63) * 16;
        const unsigned wl0 = (unsigned)((((tz >> 5) & 1) * 4 + ((tz >> 2) & 3)) * DM + nt4 * 64 + ((tz >> 4) & 1) * 32 + (tz & 3) * 8) * 2u;
        const char* aF0 = ws + WS_ACT + (size_t)(mt4 * 2 * FKS) * 1024;
        if (kq < 3) {
            if (wid == 0) {
                wait_flags(ws + WS_FA + (mt4 >> 1) * 256, 15, tag, lane);
                __builtin_amdgcn_fence(__ATOMIC_ACQUIRE, "agent");
                wait_vm<0>();
                if (lane == 0) *(volatile lu32*)((lds_ptr)lds + C_OACQ) = 1u;
#pragma unroll
                for (int j = 0; j < 3; ++j) {
                    const int o = j * 64 + lane;
                    l2_touch(aF0, (unsigned)(nt4 * 172 + (o < 172 ? o : 0)) * 128u, lds0 + L_SCR);
                }
            } else {
                lds_wait_ge(lds, C_OACQ, 1u);
            }
            lds_wait4_ge<4>(lds, C_CHAIN, (unsigned)(kq + 1));
#pragma unroll
            for (int j = 0; j < 4; ++j) {
                const int pc = qp + 4 * j;
                glds16(aF0 + (size_t)((pc >> 3) * FKS + (pc & 7) + kq * 8) * 1024, lz16, lds0 + (kq + 1) * M_STG + pc * 1024);
                glds16(w2 + (size_t)(kq * 128 + pc * 8) * DM, wl0, lds0 + (kq + 1) * M_STG + 16384 + pc * 1024);
            }
            wait_vm<0>();
        }
        const int wm = wid & 1, wn = (wid >> 1) & 1;
        const char* aF = aF0 + (size_t)((wid >> 3) * FKS + (wid & 7)) * 1024;
        const char* aO = ws + WS_O + (size_t)((mt4 * 2 + (wid >> 3)) * NH + (wid & 7)) * 1024;
        const u16* w2w = w2 + (size_t)wid * 8 * DM;
        const u16* wow = wo + (size_t)wid * 8 * DM;
        const unsigned lb = lds0 + wid * 1024;
        auto missue = [&](int s) {
            const int sa = (s == M_NS1 - 1 && (wid & 7) >= 6) ? s * 8 - 2 : s * 8;
            const int sw = (s == M_NS1 - 1 && wid >= 12) ? s * 128 - 32 : s * 128;
            glds16(aF + (size_t)sa * 1024, lz16, lb + ((s + 1) & 3) * M_STG);
            glds16(w2w + (size_t)sw * DM, wl0, lb + ((s + 1) & 3) * M_STG + 16384);
        };
        auto oissue_w = [&](int j) { glds16(wow + (size_t)j * 128 * DM, wl0, lb + ((M_NS1 + j + 1) & 3) * M_STG + 16384); };
        auto oissue_a = [&](int j) { glds16c(aO + (size_t)j * 8192, lz16, lb + ((M_NS1 + j + 1) & 3) * M_STG); };
        f32x16 acca = f32x16{}, accb = f32x16{};
        lds_cptr W = (lds_cptr)lds + 16384 + kq * 4096 + (((tz >> 5) & 1) * 2 + wn) * 256 + ((tz >> 2) & 3) * 64 + ((tz >> 4) & 1) * 32 + (tz & 3) * 8;
        lds_cptr A = (lds_cptr)lds + wm * 8192 + kq * 2048 + lz16;
        MFrag fa, fb;
        float g;
        LGKM_BAR();
        TS(8);
        missue(3);
        m_rd(fa, A + M_STG, W + M_STG);
#define MMA(FC, ACC) { SBAR(); ACC = MFMA(FC.w0, FC.a0, ACC); ACC = MFMA(FC.w1, FC.a1, ACC); PIN(ACC); SBAR(); }
#define RD(FN, S) m_rd(FN, A + (((S) + 1) & 3) * M_STG, W + (((S) + 1) & 3) * M_STG);
        LGKM_BAR(); missue(4); RD(fb, 1) MMA(fa, accb)
        LGKM_BAR(); missue(5); RD(fa, 2) MMA(fb, accb)
        wait_vm_lgkm_bar<4>(); missue(6); RD(fb, 3) MMA(fa, accb)
        raise_flag(ws + WS_FO + qg * 256 + head * 4, tag);
        wait_vm_lgkm_bar<4>(); missue(7); RD(fa, 4) MMA(fb, accb)
        wait_vm_lgkm_bar<4>(); missue(8); RD(fb, 5) MMA(fa, accb)
        wait_vm_lgkm_bar<4>(); missue(9); RD(fa, 6) MMA(fb, accb)
        wait_vm_lgkm_bar<4>(); missue(10); RD(fb, 7) MMA(fa, accb)
        wait_vm_lgkm_bar<4>(); oissue_w(0); RD(fa, 8) MMA(fb, accb)
        wait_vm_lgkm_bar<3>(); oissue_w(1); RD(fb, 9) MMA(fa, accb)
        wait_vm_lgkm_bar<2>(); oissue_w(2); RD(fa, 10) MMA(fb, accb)
        if (wid == 0) wait_flags(ws + WS_FO + (mt4 >> 2) * 256, 32, tag, lane);
        TS(11);
        LGKM_BAR();
        oissue_a(0); oissue_a(1); oissue_a(2); oissue_a(3); oissue_w(3);
        asm volatile("global_load_dword %0, %1, %2 sc1" : "=v"(g) : "v"((unsigned)(mt4 * 64 + wm * 32 + (tz & 31)) * 4u), "s"(ws + WS_G) : "memory");
        if (kq < 3) MMA(fa, accb)
        wait_vm_bar<4>(); RD(fb, 11) RD(fa, 12) MMA(fb, acca) MMA(fa, acca)
        asm volatile("s_waitcnt vmcnt(0)\n\ts_barrier" : "+v"(g) :: "memory");
        RD(fb, 13) RD(fa, 14) MMA(fb, acca) MMA(fa, acca)
#undef RD
#undef MMA
        TS(9);
        const float g1 = 1.f - g;
        {
            lds_ptr red = (lds_ptr)lds + (wid & 3) * 3072 + lz16;
#pragma unroll
            for (int d = 0; d < 4; ++d) {
                if (d != kq) *(__attribute__((address_space(3))) f32x4*)(red + d * 12288 + (kq > d ? kq - 1 : kq) * 1024) =
                    f32x4{g * acca[4 * d] + g1 * accb[4 * d], g * acca[4 * d + 1] + g1 * accb[4 * d + 1], g * acca[4 * d + 2] + g1 * accb[4 * d + 2], g * acca[4 * d + 3] + g1 * accb[4 * d + 3]};
            }
        }
        f32x4 t;
#pragma unroll
        for (int e = 0; e < 4; ++e) {
            const float a = kq == 0 ? acca[e] : kq == 1 ? acca[4 + e] : kq == 2 ? acca[8 + e] : acca[12 + e];
            const float b = kq == 0 ? accb[e] : kq == 1 ? accb[4 + e] : kq == 2 ? accb[8 + e] : accb[12 + e];
            t[e] = g * a + g1 * b;
        }
        LGKM_BAR();
        lds_cptr src = (lds_cptr)lds + (wid & 3) * 3072 + kq * 12288 + lz16;
#pragma unroll
        for (int j = 0; j < 3; ++j) t += *(const __attribute__((address_space(3))) f32x4*)(src + j * 1024);
        u32x2 p; p[0] = cvtpk(t[0], t[1]); p[1] = cvtpk(t[2], t[3]);
        *(u32x2*)(out + (size_t)(mt4 * 64 + wm * 32 + (tz & 31)) * DM + nt4 * 64 + wn * 32 + ((tz >> 5) & 1) * 4 + kq * 8) = p;
    }
    if (blockIdx.x == 0 && tid == 0) __hip_atomic_store((gu32*)(ws + WS_EP), tag, RLX_AGENT);
    TS(10);
}

extern "C" void kernel_launch(void* const* d_in, const int* in_sizes, int n_in, void* d_out, int out_size, void* d_ws, size_t ws_size,
                              hipStream_t stream) {
    (void)hipFuncSetAttribute((const void*)fused_kernel, hipFuncAttributeMaxDynamicSharedMemorySize, LDS_BYTES);
    fused_kernel<<<256, 1024, LDS_BYTES, stream>>>((const u16*)d_in[0], (const u16*)d_in[1], (const u16*)d_in[2], (const u16*)d_in[4],
                                                   (const u16*)d_in[5], (const u16*)d_in[6], (const u16*)d_in[7], (char*)d_ws, (u16*)d_out);
}
```

```cpp
#include <hip/hip_runtime.h>
#include <hip/hip_bf16.h>
#include <stdint.h>

using bf16x8 = __attribute__((ext_vector_type(8))) short;
using s16x4  = __attribute__((ext_vector_type(4))) short;
using f32x16 = __attribute__((ext_vector_type(16))) float;
using f32x4  = __attribute__((ext_vector_type(4))) float;
using f32x2  = __attribute__((ext_vector_type(2))) float;
using u32x4  = __attribute__((ext_vector_type(4))) unsigned;
using u32x2  = __attribute__((ext_vector_type(2))) unsigned;
typedef __bf16 bf16x2_t __attribute__((ext_vector_type(2)));
typedef short v4i16_t __attribute__((ext_vector_type(4)));
typedef __attribute__((address_space(3))) const char* lds_cptr;
typedef __attribute__((address_space(3))) char* lds_ptr;
typedef unsigned short u16;

#define DI __device__ __forceinline__
#define MFMA(a, b, c) __builtin_amdgcn_mfma_f32_32x32x16_bf16((a), (b), (c), 0, 0, 0)
#define SBAR() __builtin_amdgcn_sched_barrier(0)
#define PIN(x) asm volatile("" : "+v"(x))

constexpr int SEQ = 2048, DM = 512, NH = 32, DH = 16, FF = 1376, NQKV = 1536;
constexpr int NTT = SEQ / 32;
constexpr int FKS = FF / 16;
constexpr float QSCALE = 0.25f * 1.4426950408889634f;

constexpr size_t WS_Q = 0;
constexpr size_t WS_K = WS_Q + (size_t)SEQ * DM * 2;
constexpr size_t WS_V = WS_K + (size_t)SEQ * DM * 2;
constexpr size_t WS_O = WS_V + (size_t)SEQ * DM * 2;
constexpr size_t WS_ACT = WS_O + (size_t)SEQ * DM * 2;
constexpr size_t WS_KST = WS_ACT + (size_t)SEQ * FF * 2;
constexpr size_t WS_VST = WS_KST + (size_t)NH * NTT * 4;
constexpr size_t WS_QST = WS_VST + (size_t)16 * NTT * 4;
constexpr size_t WS_G = WS_QST + (size_t)NH * NTT * 4;
constexpr size_t WS_FQ = WS_G + (size_t)SEQ * 4;
constexpr size_t WS_FA = WS_FQ + 16 * 256;
constexpr size_t WS_FO = WS_FA + 16 * 256;
constexpr size_t WS_EP = WS_FO + 8 * 256;

DI unsigned cvtpk(float lo, float hi) { f32x2 v = {lo, hi}; return __builtin_bit_cast(unsigned, __builtin_convertvector(v, bf16x2_t)); }
DI float bflo(unsigned v) { return __uint_as_float(v << 16); }
DI float bfhi(unsigned v) { return __uint_as_float(v & 0xffff0000u); }
DI void glds16(const void* sbase, unsigned voff, unsigned lds_base) {
    unsigned sv;
    asm volatile("s_mov_b32 %0, m0\n\ts_mov_b32 m0, %3\n\ts_nop 0\n\tglobal_load_lds_dwordx4 %1, %2\n\ts_mov_b32 m0, %0"
                 : "=&s"(sv) : "v"(voff), "s"(sbase), "s"(lds_base) : "memory");
}
DI void glds16c(const void* sbase, unsigned voff, unsigned lds_base) {
    unsigned sv;
    asm volatile("s_mov_b32 %0, m0\n\ts_mov_b32 m0, %3\n\ts_nop 0\n\tglobal_load_lds_dwordx4 %1, %2 sc1\n\ts_mov_b32 m0, %0"
                 : "=&s"(sv) : "v"(voff), "s"(sbase), "s"(lds_base) : "memory");
}
DI s16x4 trread(lds_cptr p) { return __builtin_bit_cast(s16x4, __builtin_amdgcn_ds_read_tr16_b64_v4i16((__attribute__((address_space(3))) v4i16_t*)p)); }
DI bf16x8 ldsfrag(lds_cptr p) { return *(const __attribute__((address_space(3))) bf16x8*)p; }
DI bf16x8 cat8(s16x4 a, s16x4 b) { return __builtin_shufflevector(a, b, 0, 1, 2, 3, 4, 5, 6, 7); }
template <int N> DI void wait_vm_bar() { asm volatile("s_waitcnt vmcnt(%0)\n\ts_barrier" :: "i"(N) : "memory"); }
template <int N> DI void wait_vm_lgkm_bar() { asm volatile("s_waitcnt vmcnt(%0) lgkmcnt(0)\n\ts_barrier" :: "i"(N) : "memory"); }
DI float xor32f(float v) {
    auto r = __builtin_amdgcn_permlane32_swap(__float_as_uint(v), __float_as_uint(v), false, false);
    return (threadIdx.x & 32) ? __uint_as_float(r[0]) : __uint_as_float(r[1]);
}
DI float dpp_max63(float v) {
#define DSTEP(ctrl, rmask) v = fmaxf(v, __int_as_float(__builtin_amdgcn_update_dpp(__float_as_int(v), __float_as_int(v), ctrl, rmask, 0xf, false)))
    DSTEP(0x111, 0xf); DSTEP(0x112, 0xf); DSTEP(0x114, 0xf); DSTEP(0x118, 0xf); DSTEP(0x142, 0xa); DSTEP(0x143, 0xc);
#undef DSTEP
    return v;
}
DI float dpp_sum63(float v) {
#define DSTEP(ctrl, rmask) v += __int_as_float(__builtin_amdgcn_update_dpp(0, __float_as_int(v), ctrl, rmask, 0xf, false))
    DSTEP(0x111, 0xf); DSTEP(0x112, 0xf); DSTEP(0x114, 0xf); DSTEP(0x118, 0xf); DSTEP(0x142, 0xa); DSTEP(0x143, 0xc);
#undef DSTEP
    return v;
}
DI float mish_f(float h) {
    const float w = __builtin_amdgcn_exp2f(fminf(h * 1.4426950408889634f, 40.f));
    const float n = fmaf(w, w, w + w);
    return h * (n * __builtin_amdgcn_rcpf(n + 2.f));
}

typedef __attribute__((address_space(1))) unsigned gu32;
#define RLX_AGENT __ATOMIC_RELAXED, __HIP_MEMORY_SCOPE_AGENT
DI void st16_sc1(void* sbase, unsigned voff, u32x4 v) {
    asm volatile("global_store_dwordx4 %0, %1, %2 sc1\n\ts_nop 1" :: "v"(voff), "v"(v), "s"(sbase) : "memory");
}
DI void st4_sc1(void* p, float v) { __hip_atomic_store((gu32*)p, __float_as_uint(v), RLX_AGENT); }
DI void wait_flags(const void* flags, int n, unsigned tag, int lane) {
    for (unsigned spins = 0; spins < (1u << 18); ++spins) {
        unsigned v = tag;
        if (lane < n) v = __hip_atomic_load((const gu32*)flags + lane, RLX_AGENT);
        if (__all(v == tag)) break;
        __builtin_amdgcn_s_sleep(2);
    }
    __builtin_amdgcn_fence(__ATOMIC_ACQUIRE, "wavefront");
    asm volatile("" ::: "memory");
}

constexpr int P_BN = 96, P_ROW = P_BN * 2;
constexpr int G_XS = 16384, G_STG = G_XS + 64 * P_ROW, G_NSL = 5, LDS_BYTES = 163840;
constexpr int A_RG = 32768, A_LC = 4 * A_RG, A_CHAIN = A_LC + 2048;
constexpr int A_LQ = A_CHAIN + 20480, L_CTL = A_LQ + 8192;
constexpr int C_FAST = L_CTL, C_OACQ = L_CTL + 4, C_OCNT = L_CTL + 8, C_WOR = L_CTL + 12, C_CHAIN = L_CTL + 16, C_FULL = L_CTL + 32, C_FREE = L_CTL + 48, C_KV = L_CTL + 64, C_G2 = L_CTL + 80, C_QACQ = L_CTL + 84, C_ACNT = L_CTL + 88, C_CST = L_CTL + 92, C_XCNT = L_CTL + 96, C_QCNT = L_CTL + 100;
constexpr int M_STG = 32768, M_NS1 = (FF + 127) / 128, M_NS = M_NS1 + DM / 128;
constexpr float A_THR = 40.f;
static_assert(L_CTL >= G_NSL * G_STG && L_CTL + 128 <= LDS_BYTES - 256, "lds map");
typedef __attribute__((address_space(3))) unsigned lu32;
#define RLX_WG __ATOMIC_RELAXED, __HIP_MEMORY_SCOPE_WORKGROUP

DI bf16x8 pack8(const f32x16& p, int s) {
    u32x4 r;
#pragma unroll
    for (int q = 0; q < 4; ++q) r[q] = cvtpk(p[8 * s + 2 * q], p[8 * s + 2 * q + 1]);
    return __builtin_bit_cast(bf16x8, r);
}
#define LGKM_BAR() asm volatile("s_waitcnt lgkmcnt(0)\n\ts_barrier" ::: "memory")
#define BAR() asm volatile("s_barrier" ::: "memory")
template <int N> DI void wait_vm() { asm volatile("s_waitcnt vmcnt(%0)" :: "i"(N) : "memory"); }
DI void raise_flag(void* flag, unsigned tag) { if (threadIdx.x == 0) __hip_atomic_store((gu32*)flag, tag, RLX_AGENT); }
DI void lds_wait_ge(char* lds, int off, unsigned want) {
    asm volatile("" ::: "memory");
    while ((unsigned)__builtin_amdgcn_readfirstlane(__hip_atomic_load((lu32*)((lds_ptr)lds + off), RLX_WG)) < want) __builtin_amdgcn_s_sleep(4);
    asm volatile("" ::: "memory");
}
template <int SLEEP = 1>
DI void lds_wait4_ge(char* lds, int off, unsigned want) {
    asm volatile("" ::: "memory");
    for (;;) {
        const u32x4 v = *(volatile __attribute__((address_space(3))) u32x4*)((lds_ptr)lds + off);
        const unsigned m = min(min(v[0], v[1]), min(v[2], v[3]));
        if ((unsigned)__builtin_amdgcn_readfirstlane(m) >= want) break;
        __builtin_amdgcn_s_sleep(SLEEP);
    }
    asm volatile("" ::: "memory");
}
DI void lds_add1(char* lds, int off, int lane) {
    asm volatile("s_waitcnt lgkmcnt(0)" ::: "memory");
    if (lane == 0) __hip_atomic_fetch_add((lu32*)((lds_ptr)lds + off), 1u, RLX_WG);
}
DI void l2_touch(const void* sbase, unsigned voff, unsigned lds_scratch) {
    unsigned sv;
    asm volatile("s_mov_b32 %0, m0\n\ts_mov_b32 m0, %3\n\ts_nop 0\n\tglobal_load_lds_dword %1, %2\n\ts_mov_b32 m0, %0"
                 : "=&s"(sv) : "v"(voff), "s"(sbase), "s"(lds_scratch) : "memory");
}
DI void l2_touch64(const void* p, unsigned lds_scratch) {
    unsigned sv;
    asm volatile("s_mov_b32 %0, m0\n\ts_mov_b32 m0, %2\n\ts_nop 0\n\tglobal_load_lds_dword %1, off\n\ts_mov_b32 m0, %0"
                 : "=&s"(sv) : "v"(p), "s"(lds_scratch) : "memory");
}
constexpr int L_SCR = 163840 - 256;
struct GFrag { bf16x8 x[4]; bf16x8 w[4]; };
DI void g_rd(GFrag& f, lds_cptr b, const int (&xa)[4], int wa) {
#pragma unroll
    for (int kk = 0; kk < 4; ++kk) {
        f.x[kk] = ldsfrag(b + xa[kk]);
        f.w[kk] = cat8(trread(b + wa + kk * 16 * P_ROW), trread(b + wa + (kk * 16 + 4) * P_ROW));
    }
}
template <bool ISV> DI void g_mma(f32x16& acc, const GFrag& f) {
    SBAR();
#pragma unroll
    for (int kk = 0; kk < 4; ++kk) { if (ISV) acc = MFMA(f.x[kk], f.w[kk], acc); else acc = MFMA(f.w[kk], f.x[kk], acc); }
    PIN(acc);
    SBAR();
}
struct MFrag { bf16x8 a0, a1, w0, w1; };
DI void m_rd(MFrag& f, lds_cptr A, lds_cptr W) {
    f.w0 = cat8(trread(W), trread(W + 1024)); f.a0 = ldsfrag(A);
    f.w1 = cat8(trread(W + 2048), trread(W + 3072)); f.a1 = ldsfrag(A + 1024);
}

__global__ __launch_bounds__(1024, 4) void fused_kernel(const u16* __restrict__ x, const u16* __restrict__ wqkv, const u16* __restrict__ wo,
                                                        const u16* __restrict__ w1, const u16* __restrict__ b1, const u16* __restrict__ w2,
                                                        const u16* __restrict__ gate_w, char* __restrict__ ws, u16* __restrict__ out) {
    extern __shared__ __attribute__((aligned(16))) char lds[];
    const int tid = threadIdx.x, lane = tid & 63, wid = __builtin_amdgcn_readfirstlane(tid >> 6);
    const int r = lane & 31, hi = lane >> 5;
    const int xg = blockIdx.x & 7, idx = blockIdx.x >> 3;
    const unsigned lds0 = (unsigned)(uintptr_t)lds;
    const unsigned l16 = lane * 16;
    const int i16 = lane & 15, g4 = lane >> 4;
    const int mt = (xg >> 1) * 4 + (idx & 3), nt = (xg & 1) * 8 + (idx >> 2);
    const int head = xg * 4 + (idx & 3), qg = idx >> 2;
    const int mt4 = xg * 4 + (idx & 3), nt4 = idx >> 2;
    const int m0 = mt * 128;
    unsigned tag;

    if (wid >= 12) {
        const int lw = wid - 12;
        __builtin_amdgcn_s_setprio(3);
        const int rot = ((idx >> 2) + 2 * (idx & 3)) & 7;
        unsigned epv;
        asm volatile("global_load_dword %0, %1, %2 sc1" : "=v"(epv) : "v"(0u), "s"(ws + WS_EP) : "memory");
        const unsigned xoff = (unsigned)((m0 + lw * 32 + (lane >> 3)) * DM) * 2u + (((lane & 7) ^ g4) << 4), xoff1 = xoff ^ 64u;
        auto issue_x = [&](int s, int slot) {
            const u16* xs = x + ((s + rot) & 7) * 64;
            const unsigned base = lds0 + slot * G_STG + lw * 4096;
            glds16(xs, xoff, base);
            glds16(xs + 8 * DM, xoff1, base + 1024);
            glds16(xs + 16 * DM, xoff, base + 2048);
            glds16(xs + 24 * DM, xoff1, base + 3072);
        };
        issue_x(0, 0);
        unsigned woq[3], wof[3];
#pragma unroll
        for (int j = 0; j < 3; ++j) {
            const int o = (lw * 3 + j) * 1024 + lane * 16;
            const int k = o / P_ROW, cc = (o % P_ROW) >> 4;
            woq[j] = (unsigned)(k * NQKV + (cc >> 2) * 512 + nt * 32 + (cc & 3) * 8) * 2u;
            int n = nt * P_BN + cc * 8;
            n = n > FF - 8 ? FF - 8 : n;
            wof[j] = (unsigned)(k * FF + n) * 2u;
        }
        auto issue_w = [&](int s, int slot) {
            const int kb = (s + rot) & 7;
            const u16* wsrc = s < 8 ? wqkv + (size_t)kb * 64 * NQKV : w1 + (size_t)kb * 64 * FF;
#pragma unroll
            for (int j = 0; j < 3; ++j) glds16(wsrc, s < 8 ? woq[j] : wof[j], lds0 + slot * G_STG + G_XS + (lw * 3 + j) * 1024);
        };
        auto issue = [&](int s, int slot) { issue_x(s, slot); issue_w(s, slot); };
        issue_w(0, 0); issue(1, 1);
        asm volatile("s_waitcnt vmcnt(7)\n\ts_barrier" : "+v"(epv) :: "memory");
        tag = (unsigned)__builtin_amdgcn_readfirstlane(epv) * 0x9E3779B1u + 0x7F4A7C15u;
        issue(2, 2); issue(3, 3);
        wait_vm<14>(); BAR();
        issue(4, 4); issue(5, 0);
        {
            const int ln = idx * 192 + (lw < 3 ? lw * 64 + lane : 0), row = ln / 12;
            int col = (xg & 1) * 768 + (ln - row * 12) * 64;
            col = col > FF - 2 ? FF - 2 : col;
            l2_touch(w1, (unsigned)(row * FF + col) * 2u, lds0 + L_SCR);
        }
        wait_vm<22>(); BAR(); issue(6, 1);
        wait_vm<22>(); BAR(); issue(7, 2);
        wait_vm<22>(); BAR(); issue(8, 3);
        wait_vm<22>(); BAR(); issue(9, 4);
        int slot = 0;
#pragma unroll 1
        for (int s = 5; s < 11; ++s) {
            wait_vm<21>(); BAR();
            issue(s + G_NSL, slot);
            slot = slot + 1 == G_NSL ? 0 : slot + 1;
        }
        wait_vm<21>(); BAR();
        wait_vm<14>(); BAR();
        const void* fq = ws + WS_FQ + (head >> 1) * 256;
        bool seen = false;
        if (lw == 3) { unsigned v = tag; if (lane < 16) v = __hip_atomic_load((const gu32*)fq + lane, RLX_AGENT); seen = __all(v == tag); }
        wait_vm<7>(); BAR();
        if (lw == 3 && !seen) wait_flags(fq, 16, tag, lane);
        wait_vm<0>(); BAR();
        float kst, vst, qst;
        asm volatile("global_load_dword %0, %1, %2 sc1" : "=v"(kst) : "v"((unsigned)lane * 4u), "s"(ws + WS_KST + head * 256) : "memory");
        asm volatile("global_load_dword %0, %1, %2 sc1" : "=v"(vst) : "v"((unsigned)lane * 4u), "s"(ws + WS_VST + (head >> 1) * 256) : "memory");
        asm volatile("global_load_dword %0, %1, %2 sc1" : "=v"(qst) : "v"((unsigned)(lane & 7) * 4u), "s"(ws + WS_QST + head * 256 + qg * 32) : "memory");
        glds16c(ws + WS_Q + (size_t)(head * NTT + qg * 8 + lw * 2) * 1024, l16, lds0 + A_LQ + lw * 2048);
        glds16c(ws + WS_Q + (size_t)(head * NTT + qg * 8 + lw * 2 + 1) * 1024, l16, lds0 + A_LQ + lw * 2048 + 1024);
        const char* ksrc = ws + WS_K + (size_t)(head * NTT + lw) * 1024;
        const char* vsrc = ws + WS_V + (size_t)(head * NTT + lw) * 1024;
        auto kv = [&](int t) {
            const unsigned dst = lds0 + (((t >> 4) + 1) & 3) * A_RG + ((t & 15) + lw) * 2048;
            glds16c(ksrc + t * 1024, l16, dst);
            glds16c(vsrc + t * 1024, l16, dst + 1024);
        };
        auto kv_pub = [&](unsigned n) { if (lane == 0) *(volatile lu32*)((lds_ptr)lds + C_KV + lw * 4) = n; };
        kv(0); kv(16); kv(32);
        asm volatile("s_waitcnt vmcnt(8)" : "+v"(kst), "+v"(vst), "+v"(qst) :: "memory");
        if (lw == 0) {
            const float kmax = dpp_max63(kst), vmax = dpp_max63(vst), qmax = dpp_max63(qst);
            if (lane == 63) *(volatile lu32*)((lds_ptr)lds + C_FAST) = (qmax * kmax * 1.01f <= A_THR * A_THR && vmax <= 1e18f) ? 1u : 0u;
            asm volatile("s_waitcnt lgkmcnt(0)" ::: "memory");
        }
        lds_wait_ge(lds, C_XCNT, 12u);
        __builtin_amdgcn_s_setprio(0);
        kv(48);
        wait_vm<2>(); kv_pub(1);
        kv(4); kv(20); kv(36); kv(52);
        wait_vm<8>(); kv_pub(2);
        kv(8); kv(24); kv(40); kv(56);
        wait_vm<8>(); kv_pub(3);
        kv(12); kv(28); kv(44); kv(60);
#pragma unroll
        for (int j = 0; j < 2; ++j) {
            const int o = (lw * 2 + j) * 64 + lane;
            const int ln = idx * 472 + (o < 472 ? o : 0);
            l2_touch64(ln < 11008 ? (const char*)w2 + (size_t)ln * 128 : (const char*)wo + (size_t)(ln - 11008) * 128, lds0 + L_SCR);
        }
        wait_vm<10>(); kv_pub(4);
        wait_vm<2>(); kv_pub(5);
        __builtin_amdgcn_s_setprio(0);
    } else {
        const int tw = wid & 3, ns = wid >> 2;
        if (tid < 32) *(lu32*)((lds_ptr)lds + L_CTL + tid * 4) = 0u;
        tag = (unsigned)__builtin_amdgcn_readfirstlane(__hip_atomic_load((const gu32*)(ws + WS_EP), RLX_AGENT)) * 0x9E3779B1u + 0x7F4A7C15u;
        const int gtok = m0 + nt * 8 + (wid & 7);
        const u32x4 gx = *(const u32x4*)(x + (size_t)gtok * DM + lane * 8);
        const u32x4 gw = *(const u32x4*)(gate_w + lane * 8);
        float gs = 0.f;
#pragma unroll
        for (int q = 0; q < 4; ++q) { gs = fmaf(bflo(gx[q]), bflo(gw[q]), gs); gs = fmaf(bfhi(gx[q]), bfhi(gw[q]), gs); }
        gs = dpp_sum63(gs);
        PIN(gs);
        const int xrow = tw * 32 + r, xf7 = (xrow >> 1) & 7;
        int xa[4];
#pragma unroll
        for (int kk = 0; kk < 4; ++kk) xa[kk] = xrow * 128 + (((2 * kk + hi) ^ xf7) << 4);
        const int wa = G_XS + ((g4 >> 1) * 8 + (i16 >> 2)) * P_ROW + (ns * 32 + (g4 & 1) * 16 + (i16 & 3) * 4) * 2;
        f32x16 acc = f32x16{};
        GFrag fa, fb;
        LGKM_BAR();
        g_rd(fa, (lds_cptr)lds, xa, wa);
        int so = G_STG;
#define GSTEP(FC, FN, ISV) { LGKM_BAR(); g_rd(FN, (lds_cptr)lds + so, xa, wa); so = so + G_STG == G_NSL * G_STG ? 0 : so + G_STG; g_mma<ISV>(acc, FC); }
        if (ns == 2) {
#pragma unroll 1
            for (int s = 0; s < 8; s += 2) { GSTEP(fa, fb, true) GSTEP(fb, fa, true) }
        } else {
#pragma unroll 1
            for (int s = 0; s < 8; s += 2) { GSTEP(fa, fb, false) GSTEP(fb, fa, false) }
        }
        const int tt = mt * 4 + tw, h0 = nt * 2;
        if (ns < 2) {
            const float sc = ns == 0 ? QSCALE : 1.f;
            float ss[2];
#pragma unroll
            for (int e = 0; e < 2; ++e) {
                u32x4 p; float s2 = 0.f;
#pragma unroll
                for (int q = 0; q < 4; ++q) {
                    p[q] = cvtpk(acc[8 * e + 2 * q] * sc, acc[8 * e + 2 * q + 1] * sc);
                    const float a = bflo(p[q]), b = bfhi(p[q]);
                    s2 = fmaf(a, a, s2); s2 = fmaf(b, b, s2);
                }
                st16_sc1(ws + (ns == 0 ? WS_Q : WS_K) + (size_t)((h0 + e) * NTT + tt) * 1024, l16, p);
                ss[e] = s2;
            }
#pragma unroll
            for (int e = 0; e < 2; ++e) {
                ss[e] += xor32f(ss[e]);
                ss[e] = (ss[e] == ss[e]) ? ss[e] : __builtin_inff();
                ss[e] = dpp_max63(ss[e]);
            }
            if (lane == 63) {
                st4_sc1(ws + (ns == 0 ? WS_QST : WS_KST) + (size_t)(h0 * NTT + tt) * 4, ss[0]);
                st4_sc1(ws + (ns == 0 ? WS_QST : WS_KST) + (size_t)((h0 + 1) * NTT + tt) * 4, ss[1]);
            }
        } else {
            const unsigned vo = (unsigned)((r >> 4) * NTT * 1024 + hi * 256 + (r & 15) * 16);
            float vm = 0.f;
#pragma unroll
            for (int s2 = 0; s2 < 2; ++s2) {
                u32x4 pv;
#pragma unroll
                for (int q = 0; q < 4; ++q) {
                    const float a = acc[8 * s2 + 2 * q], b = acc[8 * s2 + 2 * q + 1];
                    pv[q] = cvtpk(a, b);
                    vm = fmaxf(vm, fmaxf((a == a) ? fabsf(a) : __builtin_inff(), (b == b) ? fabsf(b) : __builtin_inff()));
                }
                st16_sc1(ws + WS_V + (size_t)(h0 * NTT + tt) * 1024 + s2 * 512, vo, pv);
            }
            vm = dpp_max63(vm);
            if (lane == 63) st4_sc1(ws + WS_VST + (size_t)(nt * NTT + tt) * 4, vm);
        }
        if (wid < 8 && lane == 63) st4_sc1(ws + WS_G + (size_t)gtok * 4, 1.f / (1.f + __expf(-gs)));
        acc = f32x16{};
        GSTEP(fa, fb, false)
        wait_vm<0>();
        if (lane == 0 && __hip_atomic_fetch_add((lu32*)((lds_ptr)lds + C_QCNT), 1u, RLX_WG) == 11u)
            __hip_atomic_store((gu32*)(ws + WS_FQ + nt * 256 + mt * 4), tag, RLX_AGENT);
        GSTEP(fb, fa, false)
        GSTEP(fa, fb, false) GSTEP(fb, fa, false) GSTEP(fa, fb, false) GSTEP(fb, fa, false)
        u32x2 bia[2], bib[2];
        {
            int hi2 = tid; asm volatile("" : "+v"(hi2)); hi2 = (hi2 >> 5) & 1;
#pragma unroll
            for (int e = 0; e < 2; ++e) {
                int f = nt * P_BN + ns * 32 + 16 * e + 4 * hi2;
                f = f < FF ? f : 0;
                bia[e] = *(const u32x2*)(b1 + f);
                bib[e] = *(const u32x2*)(b1 + f + 8);
            }
        }
        GSTEP(fa, fb, false)
#undef GSTEP
        lds_add1(lds, C_XCNT, lane);
        if (wid == 0) {
#pragma unroll
            for (int j = 0; j < 2; ++j) {
                const unsigned one2 = ((lane & 15) == 0 || (lane & 15) == 4) ? 0x3f803f80u : 0u;
                *(__attribute__((address_space(3))) u32x4*)((lds_ptr)lds + A_LC + j * 1024 + lane * 16) = u32x4{one2, one2, one2, one2};
            }
            asm volatile("s_waitcnt lgkmcnt(0)" ::: "memory");
            if (lane == 0) *(volatile lu32*)((lds_ptr)lds + C_CST) = 1u;
        }
        g_mma<false>(acc, fb);
#pragma unroll
        for (int e = 0; e < 2; ++e) {
            const int f0 = nt * P_BN + ns * 32 + 16 * e;
            const u32x2 ba = bia[e], bb = bib[e];
            u32x4 p;
            p[0] = cvtpk(mish_f(acc[8 * e + 0] + bflo(ba[0])), mish_f(acc[8 * e + 1] + bfhi(ba[0])));
            p[1] = cvtpk(mish_f(acc[8 * e + 2] + bflo(ba[1])), mish_f(acc[8 * e + 3] + bfhi(ba[1])));
            p[2] = cvtpk(mish_f(acc[8 * e + 4] + bflo(bb[0])), mish_f(acc[8 * e + 5] + bfhi(bb[0])));
            p[3] = cvtpk(mish_f(acc[8 * e + 6] + bflo(bb[1])), mish_f(acc[8 * e + 7] + bfhi(bb[1])));
            if (f0 < FF) st16_sc1(ws + WS_ACT + (size_t)(tt * FKS + (f0 >> 4)) * 1024, l16, p);
        }
    }
    auto act_done = [&]() {
        wait_vm<0>();
        if (lane == 0 && __hip_atomic_fetch_add((lu32*)((lds_ptr)lds + C_ACNT), 1u, RLX_WG) == 11u && nt < 15)
            __hip_atomic_store((gu32*)(ws + WS_FA + mt * 256 + nt * 4), tag, RLX_AGENT);
    };
    const int qp = wid & 3, kq = wid >> 2;
    {
        lds_wait_ge(lds, C_CST, 1u);
        lds_wait4_ge<2>(lds, C_KV, kq == 3 ? 2u : 1u);
        const bf16x8 qa = ldsfrag((lds_cptr)lds + A_LQ + qp * 2048 + lane * 16), qb = ldsfrag((lds_cptr)lds + A_LQ + qp * 2048 + 1024 + lane * 16);
        const bool fast = __builtin_amdgcn_readfirstlane(*(const lu32*)((lds_cptr)lds + C_FAST)) != 0;
        f32x16 oa = f32x16{}, ob = f32x16{};
        float ma = 0.f, mb = 0.f;
        const int rg = (kq + 1) & 3;
        const int kbase = rg * A_RG + lane * 16;
        const int vbase = r < 16 ? rg * A_RG + 1024 + hi * 256 + r * 16 : A_LC + hi * 256 + (r - 16) * 16;
        const int vinc = r < 16 ? 4096 : 0;
        const int voff1 = r < 16 ? 2048 : 1024;
        const f32x16 zero = f32x16{};
        if (fast) {
            lds_cptr kc = (lds_cptr)lds + kbase, vc = (lds_cptr)lds + vbase, vd = vc + voff1;
            bf16x8 kf = ldsfrag(kc);
            bf16x8 va0 = ldsfrag(vc), va1 = ldsfrag(vc + 512), vb0, vb1;
            f32x16 sa = MFMA(kf, qa, zero), sb = MFMA(kf, qb, zero);
            kf = ldsfrag(kc + 2048);
#define HALF(S, O, V0, V1)                                                              \
                _Pragma("unroll")                                                       \
                for (int i = 0; i < 8; ++i) S[i] = __builtin_amdgcn_exp2f(S[i]);        \
                O = MFMA(V0, pack8(S, 0), O);                                           \
                PIN(O); SBAR();                                                         \
                _Pragma("unroll")                                                       \
                for (int i = 8; i < 16; ++i) S[i] = __builtin_amdgcn_exp2f(S[i]);       \
                O = MFMA(V1, pack8(S, 1), O);                                           \
                PIN(O); SBAR();
#define TILE(VC0, VC1, VN0, VN1, KNEXT, VNEXT)                                          \
            {                                                                           \
                VN0 = ldsfrag(VNEXT); VN1 = ldsfrag((VNEXT) + 512);                     \
                SBAR();                                                                 \
                HALF(sa, oa, VC0, VC1)                                                  \
                sa = MFMA(kf, qa, zero); PIN(sa); SBAR();                               \
                HALF(sb, ob, VC0, VC1)                                                  \
                sb = MFMA(kf, qb, zero); PIN(sb);                                       \
                kf = ldsfrag(KNEXT);                                                    \
                SBAR();                                                                 \
            }
#pragma unroll 1
            for (int t = 0; t < 16; t += 2) {
                if (t == 0) __builtin_amdgcn_s_setprio(3); else if (t == 4) __builtin_amdgcn_s_setprio(2); else if (t == 8) __builtin_amdgcn_s_setprio(1); else if (t == 12) __builtin_amdgcn_s_setprio(0);
                if ((t & 3) == 2 && t < 14) lds_wait4_ge(lds, C_KV, (unsigned)((t >> 2) + 3));
                if (t == 6 && kq < 3) act_done();
                const int kn = t < 14 ? 4096 : 0;
                TILE(va0, va1, vb0, vb1, kc + kn, vd)
                vc += t < 14 ? vinc : 0;
                TILE(vb0, vb1, va0, va1, kc + kn + 2048, vc)
                kc += kn; vd += vinc;
            }
#undef TILE
#undef HALF
        } else {
            ma = -__builtin_inff(); mb = ma;
#pragma unroll 1
            for (int t = 0; t < 16; ++t) {
                if ((t & 3) == 0 && t > 0) lds_wait4_ge(lds, C_KV, (unsigned)((t >> 2) + 2));
                if (t == 6 && kq < 3) act_done();
                lds_cptr vc = (lds_cptr)lds + vbase + (r < 16 ? t * 2048 : 0);
                const bf16x8 kf = ldsfrag((lds_cptr)lds + kbase + t * 2048);
                const bf16x8 v0 = ldsfrag(vc), v1 = ldsfrag(vc + 512);
#define SLOW(Q, O, M)                                                                   \
                {                                                                       \
                    f32x16 s = MFMA(kf, Q, zero);                                       \
                    float mx = s[0];                                                    \
                    _Pragma("unroll")                                                   \
                    for (int i = 1; i < 16; ++i) mx = fmaxf(mx, s[i]);                  \
                    mx = fmaxf(mx, xor32f(mx));                                         \
                    const float mn = fmaxf(M, mx);                                      \
                    const float al = __builtin_amdgcn_exp2f(M - mn);                    \
                    M = mn;                                                             \
                    _Pragma("unroll")                                                   \
                    for (int i = 0; i < 16; ++i) { s[i] = __builtin_amdgcn_exp2f(s[i] - mn); O[i] *= al; } \
                    O = MFMA(v0, pack8(s, 0), O);                                       \
                    O = MFMA(v1, pack8(s, 1), O);                                       \
                }
                SLOW(qa, oa, ma)
                SLOW(qb, ob, mb)
#undef SLOW
            }
        }
        __builtin_amdgcn_s_setprio(3);
        {
            int tz = tid; asm volatile("" : "+v"(tz));
            lds_ptr ch = (lds_ptr)lds + A_CHAIN + qp * 5120 + (tz & 63) * 16;
            lds_ptr cl = (lds_ptr)lds + A_CHAIN + qp * 5120 + 4096 + (tz & 63) * 16;
            if (kq > 0) {
                lds_wait_ge(lds, C_CHAIN + qp * 4, (unsigned)kq);
                const f32x4 a0 = *(__attribute__((address_space(3))) f32x4*)(ch), a1 = *(__attribute__((address_space(3))) f32x4*)(ch + 1024);
                const f32x4 b0 = *(__attribute__((address_space(3))) f32x4*)(ch + 2048), b1v = *(__attribute__((address_space(3))) f32x4*)(ch + 3072);
                const f32x4 lm = *(__attribute__((address_space(3))) f32x4*)(cl);
                const float na = fmaxf(ma, lm[1]), nb = fmaxf(mb, lm[3]);
                const float fa = __builtin_amdgcn_exp2f(ma - na), xa2 = __builtin_amdgcn_exp2f(lm[1] - na);
                const float fb = __builtin_amdgcn_exp2f(mb - nb), xb2 = __builtin_amdgcn_exp2f(lm[3] - nb);
#pragma unroll
                for (int e = 0; e < 4; ++e) {
                    oa[e] = fmaf(a0[e], xa2, oa[e] * fa); oa[4 + e] = fmaf(a1[e], xa2, oa[4 + e] * fa);
                    ob[e] = fmaf(b0[e], xb2, ob[e] * fb); ob[4 + e] = fmaf(b1v[e], xb2, ob[4 + e] * fb);
                }
                oa[8] = fmaf(lm[0], xa2, oa[8] * fa); ob[8] = fmaf(lm[2], xb2, ob[8] * fb);
                ma = na; mb = nb;
            }
            if (kq < 3) {
                *(__attribute__((address_space(3))) f32x4*)(ch) = f32x4{oa[0], oa[1], oa[2], oa[3]};
                *(__attribute__((address_space(3))) f32x4*)(ch + 1024) = f32x4{oa[4], oa[5], oa[6], oa[7]};
                *(__attribute__((address_space(3))) f32x4*)(ch + 2048) = f32x4{ob[0], ob[1], ob[2], ob[3]};
                *(__attribute__((address_space(3))) f32x4*)(ch + 3072) = f32x4{ob[4], ob[5], ob[6], ob[7]};
                *(__attribute__((address_space(3))) f32x4*)(cl) = f32x4{oa[8], ma, ob[8], mb};
                lds_add1(lds, C_CHAIN + qp * 4, lane);
            } else {
                const float ra = 1.f / oa[8], rb = 1.f / ob[8];
                u32x4 pa, pb;
#pragma unroll
                for (int q = 0; q < 4; ++q) { pa[q] = cvtpk(oa[2 * q] * ra, oa[2 * q + 1] * ra); pb[q] = cvtpk(ob[2 * q] * rb, ob[2 * q + 1] * rb); }
                const int qt = qg * 8 + qp * 2;
                st16_sc1(ws + WS_O + (size_t)(qt * NH + head) * 1024, l16, pa);
                st16_sc1(ws + WS_O + (size_t)((qt + 1) * NH + head) * 1024, l16, pb);
            }
        }
        __builtin_amdgcn_s_setprio(0);
    }
    {
        int tz = tid; asm volatile("" : "+v"(tz));
        const unsigned lz16 = (tz & 63) * 16;
        const unsigned wl0 = (unsigned)((((tz >> 5) & 1) * 4 + ((tz >> 2) & 3)) * DM + nt4 * 64 + ((tz >> 4) & 1) * 32 + (tz & 3) * 8) * 2u;
        const char* aF0 = ws + WS_ACT + (size_t)(mt4 * 2 * FKS) * 1024;
        if (kq < 3) {
            if (wid == 0) {
                wait_flags(ws + WS_FA + (mt4 >> 1) * 256, 15, tag, lane);
                __builtin_amdgcn_fence(__ATOMIC_ACQUIRE, "agent");
                wait_vm<0>();
                if (lane == 0) *(volatile lu32*)((lds_ptr)lds + C_OACQ) = 1u;
#pragma unroll
                for (int j = 0; j < 3; ++j) {
                    const int o = j * 64 + lane;
                    l2_touch(aF0, (unsigned)(nt4 * 172 + (o < 172 ? o : 0)) * 128u, lds0 + L_SCR);
                }
            } else {
                lds_wait_ge(lds, C_OACQ, 1u);
            }
            lds_wait4_ge<4>(lds, C_CHAIN, (unsigned)(kq + 1));
#pragma unroll
            for (int j = 0; j < 4; ++j) {
                const int pc = qp + 4 * j;
                glds16(aF0 + (size_t)((pc >> 3) * FKS + (pc & 7) + kq * 8) * 1024, lz16, lds0 + (kq + 1) * M_STG + pc * 1024);
                glds16(w2 + (size_t)(kq * 128 + pc * 8) * DM, wl0, lds0 + (kq + 1) * M_STG + 16384 + pc * 1024);
            }
            wait_vm<0>();
        }
        const int wm = wid & 1, wn = (wid >> 1) & 1;
        const char* aF = aF0 + (size_t)((wid >> 3) * FKS + (wid & 7)) * 1024;
        const char* aO = ws + WS_O + (size_t)((mt4 * 2 + (wid >> 3)) * NH + (wid & 7)) * 1024;
        const u16* w2w = w2 + (size_t)wid * 8 * DM;
        const u16* wow = wo + (size_t)wid * 8 * DM;
        const unsigned lb = lds0 + wid * 1024;
        auto missue = [&](int s) {
            const int sa = (s == M_NS1 - 1 && (wid & 7) >= 6) ? s * 8 - 2 : s * 8;
            const int sw = (s == M_NS1 - 1 && wid >= 12) ? s * 128 - 32 : s * 128;
            glds16(aF + (size_t)sa * 1024, lz16, lb + ((s + 1) & 3) * M_STG);
            glds16(w2w + (size_t)sw * DM, wl0, lb + ((s + 1) & 3) * M_STG + 16384);
        };
        auto oissue_w = [&](int j) { glds16(wow + (size_t)j * 128 * DM, wl0, lb + ((M_NS1 + j + 1) & 3) * M_STG + 16384); };
        auto oissue_a = [&](int j) { glds16c(aO + (size_t)j * 8192, lz16, lb + ((M_NS1 + j + 1) & 3) * M_STG); };
        f32x16 acca = f32x16{}, accb = f32x16{};
        lds_cptr W = (lds_cptr)lds + 16384 + kq * 4096 + (((tz >> 5) & 1) * 2 + wn) * 256 + ((tz >> 2) & 3) * 64 + ((tz >> 4) & 1) * 32 + (tz & 3) * 8;
        lds_cptr A = (lds_cptr)lds + wm * 8192 + kq * 2048 + lz16;
        MFrag fa, fb;
        float g;
        LGKM_BAR();
        missue(3);
        m_rd(fa, A + M_STG, W + M_STG);
#define MMA(FC, ACC) { SBAR(); ACC = MFMA(FC.w0, FC.a0, ACC); ACC = MFMA(FC.w1, FC.a1, ACC); PIN(ACC); SBAR(); }
#define RD(FN, S) m_rd(FN, A + (((S) + 1) & 3) * M_STG, W + (((S) + 1) & 3) * M_STG);
        LGKM_BAR(); missue(4); RD(fb, 1) MMA(fa, accb)
        LGKM_BAR(); missue(5); RD(fa, 2) MMA(fb, accb)
        wait_vm_lgkm_bar<4>(); missue(6); RD(fb, 3) MMA(fa, accb)
        raise_flag(ws + WS_FO + qg * 256 + head * 4, tag);
        wait_vm_lgkm_bar<4>(); missue(7); RD(fa, 4) MMA(fb, accb)
        wait_vm_lgkm_bar<4>(); missue(8); RD(fb, 5) MMA(fa, accb)
        wait_vm_lgkm_bar<4>(); missue(9); RD(fa, 6) MMA(fb, accb)
        wait_vm_lgkm_bar<4>(); missue(10); RD(fb, 7) MMA(fa, accb)
        wait_vm_lgkm_bar<4>(); oissue_w(0); RD(fa, 8) MMA(fb, accb)
        wait_vm_lgkm_bar<3>(); oissue_w(1); RD(fb, 9) MMA(fa, accb)
        wait_vm_lgkm_bar<2>(); oissue_w(2); RD(fa, 10) MMA(fb, accb)
        if (wid == 0) wait_flags(ws + WS_FO + (mt4 >> 2) * 256, 32, tag, lane);
        LGKM_BAR();
        oissue_a(0); oissue_a(1); oissue_a(2); oissue_a(3); oissue_w(3);
        asm volatile("global_load_dword %0, %1, %2 sc1" : "=v"(g) : "v"((unsigned)(mt4 * 64 + wm * 32 + (tz & 31)) * 4u), "s"(ws + WS_G) : "memory");
        if (kq < 3) MMA(fa, accb)
        wait_vm_bar<4>(); RD(fb, 11) RD(fa, 12) MMA(fb, acca) MMA(fa, acca)
        asm volatile("s_waitcnt vmcnt(0)\n\ts_barrier" : "+v"(g) :: "memory");
        RD(fb, 13) RD(fa, 14) MMA(fb, acca) MMA(fa, acca)
#undef RD
#undef MMA
        const float g1 = 1.f - g;
        {
            lds_ptr red = (lds_ptr)lds + (wid & 3) * 3072 + lz16;
#pragma unroll
            for (int d = 0; d < 4; ++d) {
                if (d != kq) *(__attribute__((address_space(3))) f32x4*)(red + d * 12288 + (kq > d ? kq - 1 : kq) * 1024) =
                    f32x4{g * acca[4 * d] + g1 * accb[4 * d], g * acca[4 * d + 1] + g1 * accb[4 * d + 1], g * acca[4 * d + 2] + g1 * accb[4 * d + 2], g * acca[4 * d + 3] + g1 * accb[4 * d + 3]};
            }
        }
        f32x4 t;
#pragma unroll
        for (int e = 0; e < 4; ++e) {
            const float a = kq == 0 ? acca[e] : kq == 1 ? acca[4 + e] : kq == 2 ? acca[8 + e] : acca[12 + e];
            const float b = kq == 0 ? accb[e] : kq == 1 ? accb[4 + e] : kq == 2 ? accb[8 + e] : accb[12 + e];
            t[e] = g * a + g1 * b;
        }
        LGKM_BAR();
        lds_cptr src = (lds_cptr)lds + (wid & 3) * 3072 + kq * 12288 + lz16;
#pragma unroll
        for (int j = 0; j < 3; ++j) t += *(const __attribute__((address_space(3))) f32x4*)(src + j * 1024);
        u32x2 p; p[0] = cvtpk(t[0], t[1]); p[1] = cvtpk(t[2], t[3]);
        *(u32x2*)(out + (size_t)(mt4 * 64 + wm * 32 + (tz & 31)) * DM + nt4 * 64 + wn * 32 + ((tz >> 5) & 1) * 4 + kq * 8) = p;
    }
    if (blockIdx.x == 0 && tid == 0) __hip_atomic_store((gu32*)(ws + WS_EP), tag, RLX_AGENT);
}

extern "C" void kernel_launch(void* const* d_in, const int* in_sizes, int n_in, void* d_out, int out_size, void* d_ws, size_t ws_size,
                              hipStream_t stream) {
    (void)hipFuncSetAttribute((const void*)fused_kernel, hipFuncAttributeMaxDynamicSharedMemorySize, LDS_BYTES);
    fused_kernel<<<256, 1024, LDS_BYTES, stream>>>((const u16*)d_in[0], (const u16*)d_in[1], (const u16*)d_in[2], (const u16*)d_in[4],
                                                   (const u16*)d_in[5], (const u16*)d_in[6], (const u16*)d_in[7], (char*)d_ws, (u16*)d_out);
}
```
